```python
import torch

def _softmax(x, axis=-1):
    return torch.softmax(x, dim=axis)
def _gelu(x):
    return torch.nn.functional.gelu(x, approximate="tanh")
def _silu(x):
    return torch.nn.functional.silu(x)
def _sigmoid(x):
    return torch.sigmoid(x)
def _layernorm(x, axis=-1, eps=1e-5):
    m = x.mean(dim=axis, keepdim=True)
    v = x.var(dim=axis, keepdim=True, unbiased=False)
    return (x - m) / torch.sqrt(v + eps)
def _scatter_add(tgt, idx, src, axis=0):
    return tgt.clone().index_add_(axis, idx, src)
def _segment_sum(src, seg, n):
    out = torch.zeros((n,) + src.shape[1:], dtype=src.dtype)
    return out.index_add_(0, seg, src)
def _pad(x, pw):
    flat = tuple(v for pair in reversed(pw) for v in pair)
    return torch.nn.functional.pad(x, flat)

kernel_name = 'parallel_sum__sliding_local_dilated__glu_gated_geglu__prompt'

def setup_inputs():

    _w = (lambda a: (lambda t: t.to(torch.bfloat16) if t.is_floating_point() else t)(torch.as_tensor(a)))
    def _A_setup():
        B, S, D = 1, 2048, 640
        return {
            "x": _w((lambda *s: torch.randn(*s, dtype=torch.bfloat16) * 0.05)(B, S, D)),
            "wqkv": _w((lambda *s: torch.randn(*s, dtype=torch.bfloat16) * 0.05)(D, 3 * D)),
            "wo": _w((lambda *s: torch.randn(*s, dtype=torch.bfloat16) * 0.05)(D, D)),
        }
    _A_in = _A_setup()

    def _B_setup():
        B, S, Dm, Df = 1, 2048, 640, 1536
        return {
            "x": _w((lambda *s: torch.randn(*s, dtype=torch.bfloat16) * 0.05)(B, S, Dm)),
            "w_gate": _w((lambda *s: torch.randn(*s, dtype=torch.bfloat16) * 0.8)(Dm, Df) * (Dm ** -0.5)),
            "w_up": _w((lambda *s: torch.randn(*s, dtype=torch.bfloat16) * 0.8)(Dm, Df) * (Dm ** -0.5)),
            "w_down": _w((lambda *s: torch.randn(*s, dtype=torch.bfloat16) * 0.8)(Df, Dm) * (Df ** -0.5)),
        }
    _B_in = _B_setup()

    return {
        "A_x": _A_in["x"],
        "A_wqkv": _A_in["wqkv"],
        "A_wo": _A_in["wo"],
        "B_x": _B_in["x"],
        "B_w_gate": _B_in["w_gate"],
        "B_w_up": _B_in["w_up"],
        "B_w_down": _B_in["w_down"],
    }

def reference(A_x, A_wqkv, A_wo, B_x, B_w_gate, B_w_up, B_w_down):
    def _A_ref(x, wqkv, wo):
        B, S, D = x.shape
        H = 32; Dh = D // H
        W, dil = 8, 2
        qkv = (lambda x, *d: x.permute(*d))((x @ wqkv).reshape(B, S, 3, H, Dh), 2, 0, 3, 1, 4)
        q, k, v = qkv[0], qkv[1], qkv[2]
        scale = Dh ** -0.5
        pos = torch.arange(S)
        off = torch.arange(W) * dil
        idx = pos[:, None] - off[None, :]
        mask = idx >= 0
        idx_c = torch.where(mask, idx, 0).reshape(-1)
        kg = k[:, :, idx_c, :].reshape(B, H, S, W, Dh)
        vg = v[:, :, idx_c, :].reshape(B, H, S, W, Dh)
        logits = torch.einsum("bhsd,bhswd->bhsw", q, kg) * scale
        logits = torch.where(mask[None, None, :, :], logits, -1e9)
        attn = _softmax(logits, axis=-1)
        o = torch.einsum("bhsw,bhswd->bhsd", attn, vg)
        return (lambda x, *d: x.permute(*d))(o, 0, 2, 1, 3).reshape(B, S, D) @ wo

    def _B_ref(x, w_gate, w_up, w_down):
        g = torch.einsum("bsd,df->bsf", x, w_gate)
        u = torch.einsum("bsd,df->bsf", x, w_up)
        h = _gelu(g) * u
        return torch.einsum("bsf,fd->bsd", h, w_down)

    x = A_x
    xn = _layernorm(x, axis=-1)
    return x + _A_ref(xn, A_wqkv, A_wo) + _B_ref(xn, B_w_gate, B_w_up, B_w_down)
```

```cpp
#include <hip/hip_runtime.h>
#include <hip/hip_bf16.h>
#include <stdint.h>
typedef unsigned short u16;
__device__ __forceinline__ float b2f(u16 v){ return __uint_as_float(((unsigned)v)<<16); }
__device__ __forceinline__ u16 f2b(float f){ unsigned u=__float_as_uint(f); u += 0x7fffu + ((u>>16)&1u); return (u16)(u>>16); }
constexpr int S=2048, D=640, DF=1536, H=32, DH=20;

__global__ void k_ln(const u16* x, u16* xn){
  int r=blockIdx.x; int t=threadIdx.x;
  float s=0, ss=0; float v[10];
  for(int i=0;i<10;i++){ v[i]=b2f(x[r*D+t+64*i]); s+=v[i]; }
  for(int o=32;o>0;o>>=1) s+=__shfl_xor(s,o);
  float m=s/D;
  for(int i=0;i<10;i++){ float d=v[i]-m; ss+=d*d; }
  for(int o=32;o>0;o>>=1) ss+=__shfl_xor(ss,o);
  float rs=rsqrtf(ss/D+1e-5f);
  for(int i=0;i<10;i++) xn[r*D+t+64*i]=f2b((v[i]-m)*rs);
}
__global__ void k_gemm(const u16* A, const u16* B, float* C, int M,int N,int K,int lda){
  __shared__ float As[16][17], Bs[16][17];
  int tx=threadIdx.x&15, ty=threadIdx.x>>4;
  int row=blockIdx.y*16+ty, col=blockIdx.x*16+tx;
  float acc=0;
  for(int k0=0;k0<K;k0+=16){
    As[ty][tx]=b2f(A[row*lda+k0+tx]);
    Bs[ty][tx]=b2f(B[(k0+ty)*N+col]);
    __syncthreads();
    for(int k=0;k<16;k++) acc+=As[ty][k]*Bs[k][tx];
    __syncthreads();
  }
  C[row*N+col]=acc;
}
__global__ void k_attn(const float* qkv, u16* o){
  int idx=blockIdx.x*blockDim.x+threadIdx.x; int s=idx/H, h=idx%H;
  float q[DH]; for(int d=0;d<DH;d++) q[d]=b2f(f2b(qkv[s*1920+h*DH+d]));
  float l[8]; float mx=-1e30f;
  for(int w=0;w<8;w++){ int p=s-2*w; if(p<0){ l[w]=-1e9f; } else { float a=0; for(int d=0;d<DH;d++) a+=q[d]*b2f(f2b(qkv[p*1920+640+h*DH+d])); l[w]=a*0.22360679774997896f; } mx=fmaxf(mx,l[w]); }
  float sum=0; for(int w=0;w<8;w++){ l[w]=__expf(l[w]-mx); sum+=l[w]; }
  for(int d=0;d<DH;d++){ float a=0; for(int w=0;w<8;w++){ int p=s-2*w; if(p<0)p=0; a+=b2f(f2b(l[w]/sum))*b2f(f2b(qkv[p*1920+1280+h*DH+d])); } o[s*D+h*DH+d]=f2b(a); }
}
__global__ void k_geglu(const float* g,const float* u,u16* hbuf){
  int i=blockIdx.x*blockDim.x+threadIdx.x; float x=g[i];
  float t=tanhf(0.7978845608028654f*(x+0.044715f*x*x*x));
  hbuf[i]=f2b(0.5f*x*(1.f+t)*u[i]);
}
__global__ void k_final(const u16* x,const float* a,const float* b,u16* out){
  int i=blockIdx.x*blockDim.x+threadIdx.x; out[i]=f2b(b2f(x[i])+a[i]+b[i]);
}
extern "C" void kernel_launch(void* const* d_in, const int* in_sizes, int n_in, void* d_out, int out_size, void* d_ws, size_t ws_size, hipStream_t stream) {
  const u16* x=(const u16*)d_in[0]; const u16* wqkv=(const u16*)d_in[1]; const u16* wo=(const u16*)d_in[2];
  const u16* wg=(const u16*)d_in[4]; const u16* wu=(const u16*)d_in[5]; const u16* wd=(const u16*)d_in[6];
  char* ws=(char*)d_ws;
  u16* xn=(u16*)ws; ws+=S*D*2;
  float* qkv=(float*)ws; ws+=S*1920*4;
  float* g=(float*)ws; ws+=S*DF*4;
  float* u=(float*)ws; ws+=S*DF*4;
  u16* o=(u16*)ws; ws+=S*D*2;
  u16* hb=(u16*)ws; ws+=S*DF*2;
  float* a=(float*)ws; ws+=S*D*4;
  float* b=(float*)ws; ws+=S*D*4;
  k_ln<<<S,64,0,stream>>>(x,xn);
  k_gemm<<<dim3(1920/16,S/16),256,0,stream>>>(xn,wqkv,qkv,S,1920,D,D);
  k_gemm<<<dim3(DF/16,S/16),256,0,stream>>>(xn,wg,g,S,DF,D,D);
  k_gemm<<<dim3(DF/16,S/16),256,0,stream>>>(xn,wu,u,S,DF,D,D);
  k_attn<<<S*H/256,256,0,stream>>>(qkv,o);
  k_geglu<<<S*DF/256,256,0,stream>>>(g,u,hb);
  k_gemm<<<dim3(D/16,S/16),256,0,stream>>>(o,wo,a,S,D,D,D);
  k_gemm<<<dim3(D/16,S/16),256,0,stream>>>(hb,wd,b,S,D,DF,DF);
  k_final<<<S*D/256,256,0,stream>>>(x,a,b,(u16*)d_out);
}
```

```cpp
#include <hip/hip_runtime.h>
#include <stdint.h>

#define LAS __attribute__((address_space(3)))
#define DI __device__ __forceinline__
typedef short s16x4 __attribute__((ext_vector_type(4)));
typedef __bf16 b16x2 __attribute__((ext_vector_type(2)));
typedef __bf16 b16x8 __attribute__((ext_vector_type(8)));
typedef float f32x2 __attribute__((ext_vector_type(2)));
typedef float f32x4 __attribute__((ext_vector_type(4)));
typedef unsigned u32x2 __attribute__((ext_vector_type(2)));
typedef unsigned u32x4 __attribute__((ext_vector_type(4)));

constexpr int LDS_BYTES = 163840;
constexpr int XBUF = 20480, EB0 = 61440, EBUF = 1536, WB0 = 65536, WBUF = 32768;
constexpr int ST_OFF = 0;
constexpr int CS_OFF = 3072;
constexpr int KX_OFF = 4096;
constexpr int V_OFF = 8192;
constexpr int O_OFF = 43008;

DI unsigned pk2(float a, float b) { f32x2 v = {a, b}; return __builtin_bit_cast(unsigned, __builtin_convertvector(v, b16x2)); }
DI f32x4 mfma(b16x8 a, b16x8 b, f32x4 c) { return __builtin_amdgcn_mfma_f32_16x16x32_bf16(a, b, c, 0, 0, 0); }
DI b16x8 lds_b128(LAS char* p) { return *(LAS b16x8*)p; }
DI b16x8 lds_tr2(LAS char* p, int d) {
  s16x4 a = __builtin_amdgcn_ds_read_tr16_b64_v4i16((LAS s16x4*)p);
  s16x4 b = __builtin_amdgcn_ds_read_tr16_b64_v4i16((LAS s16x4*)(p + d));
  return __builtin_bit_cast(b16x8, __builtin_shufflevector(a, b, 0, 1, 2, 3, 4, 5, 6, 7));
}
DI void dma16(const char* g, unsigned dst) {
  asm volatile("s_mov_b32 m0, %1\n\ts_nop 0\n\tglobal_load_lds_dwordx4 %0, off" ::"v"(g), "s"(dst) : "memory");
}
DI void dma16_masked(const char* g, unsigned dst, unsigned long long mask) {
  asm volatile("s_mov_b32 m0, %1\n\ts_mov_b64 exec, %2\n\tglobal_load_lds_dwordx4 %0, off\n\ts_mov_b64 exec, -1" ::"v"(g), "s"(dst), "s"(mask) : "memory");
}
#define WAIT_VM(n) asm volatile("s_waitcnt vmcnt(" #n ")" ::: "memory")
#define WAIT_LGKM0() asm volatile("s_waitcnt lgkmcnt(0)" ::: "memory")
#define BAR() __builtin_amdgcn_s_barrier()
DI int tok_of_slot(int s) { return s < 1024 ? 2 * s : 2 * s - 2047; }
DI float xrow_max(float v) {
  unsigned u = __float_as_uint(v);
  auto r = __builtin_amdgcn_permlane16_swap(u, u, false, false);
  v = fmaxf(__uint_as_float(r[0]), __uint_as_float(r[1]));
  u = __float_as_uint(v);
  auto r2 = __builtin_amdgcn_permlane32_swap(u, u, false, false);
  return fmaxf(__uint_as_float(r2[0]), __uint_as_float(r2[1]));
}
DI float xrow_sum(float v) {
  unsigned u = __float_as_uint(v);
  auto r = __builtin_amdgcn_permlane16_swap(u, u, false, false);
  v = __uint_as_float(r[0]) + __uint_as_float(r[1]);
  u = __float_as_uint(v);
  auto r2 = __builtin_amdgcn_permlane32_swap(u, u, false, false);
  return __uint_as_float(r2[0]) + __uint_as_float(r2[1]);
}
DI void stat_acc(b16x8 f, float& s, float& q) {
  u32x4 d = __builtin_bit_cast(u32x4, f);
#pragma unroll
  for (int i = 0; i < 4; i++) {
    float lo = __uint_as_float(d[i] << 16), hi = __uint_as_float(d[i] & 0xffff0000u);
    s += lo; q = fmaf(lo, lo, q);
    s += hi; q = fmaf(hi, hi, q);
  }
}

struct Frag { b16x8 x[6]; b16x8 w[4]; };

__global__ __launch_bounds__(512, 2) void k_phase1(const char* __restrict__ X, const char* __restrict__ Wqkv, const char* __restrict__ Wg,
                                                   const char* __restrict__ Wu, char* __restrict__ H) {
  __shared__ __attribute__((aligned(16))) char smem_[LDS_BYTES];
  LAS char* smem = (LAS char*)smem_;
  const int tid = threadIdx.x, l = tid & 63;
  const int w = __builtin_amdgcn_readfirstlane(tid >> 6), s = w & 3;
  const bool isA = w < 4;
  const int g = l >> 4, c = l & 15, q = c >> 2, p = l & 3;
  const int bid = blockIdx.x, xj = bid & 7, s32 = bid >> 3;
  if (s32 == 31) return;
  const bool ffn = s32 < 18;
  int t0, tend, grp;
  if (ffn) {
    grp = 3 * (xj >> 1) + s32 % 3;
    int tt = 6 * (xj & 1) + s32 / 3;
    t0 = (2048 * tt) / 12; tend = (2048 * (tt + 1)) / 12;
  } else {
    int idx = s32 - 18, tile;
    if ((xj & 1) == 0) { grp = 2 * (xj >> 1) + (idx >= 7); tile = idx >= 7 ? idx - 7 : idx; }
    else { grp = 2 * (xj >> 1) + (idx >= 6); tile = idx >= 6 ? idx : idx + 7; }
    t0 = 160 * tile; tend = min(t0 + 160, 2048);
  }
  const char* ga[7];
  unsigned dst[7];
  bool ext0 = w >= 6;
  {
#pragma unroll
    for (int i = 0; i < 3; i++) {
      int b, hf;
      bool e = false;
      if (w < 6) { int pc = 3 * w + i; b = pc >> 1; hf = pc & 1; }
      else if (i == 0) { e = true; b = 10; hf = w - 6; }
      else if (w == 6) { b = 9; hf = i - 1; }
      else { b = 9; hf = i - 1; }
      int r = 8 * hf + (l >> 3);
      int slot = e ? (ffn ? t0 + 155 + r : t0 - 16 + r) : t0 + 16 * b + r;
      slot = min(max(slot, 0), 2047);
      int sc = (l & 7) ^ ((r >> 1) & 7);
      ga[i] = X + (size_t)tok_of_slot(slot) * 1280 + sc * 16;
      dst[i] = e ? EB0 - 512 + hf * 1024 : b * 2048 + hf * 1024;
    }
#pragma unroll
    for (int i = 0; i < 4; i++) {
      int r2 = 4 * w + i, k = 2 * r2 + (l >> 5);
      int f = (k & 3) | (((k >> 3) & 1) << 2);
      int lc = (l & 31) ^ (f << 1);
      const char* a;
      if (ffn) a = (lc < 16 ? Wg : Wu) + (size_t)k * 3072 + grp * 256 + (lc & 15) * 16;
      else {
        int m = lc < 10 ? 0 : lc < 21 ? 1 : 2;
        int cc = lc < 10 ? lc : lc < 20 ? lc - 10 : lc == 20 ? 9 : lc < 31 ? lc - 21 : 9;
        a = Wqkv + (size_t)k * 3840 + m * 1280 + grp * 160 + cc * 16;
      }
      ga[3 + i] = a;
      dst[3 + i] = WB0 + r2 * 1024;
    }
  }
  const unsigned long long mask0 = (w == 6) ? 0xFFFFFF0000000000ull : ~0ull;
  const size_t wstep = ffn ? 64 * 3072 : 64 * 3840;
  auto issue = [&](int xo, int eo, int wo, bool first) {
    if (first) { dma16_masked(ga[0], dst[0] + (ext0 ? eo : xo), mask0); ga[0] += 128; }
    dma16(ga[1], dst[1] + xo); ga[1] += 128;
    dma16(ga[2], dst[2] + xo); ga[2] += 128;
#pragma unroll
    for (int i = 3; i < 7; i++) { dma16(ga[i], dst[i] + wo); ga[i] += wstep; }
  };
  issue(0, 0, 0, true);
  issue(XBUF, EBUF, WBUF, true);
  issue(2 * XBUF, 0, 2 * WBUF, !ext0);
  if (tid < 64) *(LAS unsigned*)(smem + EB0 + (tid >> 5) * EBUF + (tid & 31) * 4) = 0x3F803F80u;

  int xr[2], er[2], sr0[2], sr1[2], wr[4];
  {
    int b0 = c * 128 + ((g ^ (c >> 1)) << 4);
    int re = max(c, 4);
    int e0 = EB0 + (re - 4) * 128 + ((g ^ (re >> 1)) << 4);
    xr[0] = b0 + (isA ? 0 : 5 * 2048); xr[1] = xr[0] ^ 64;
    er[0] = e0; er[1] = e0 ^ 64;
    sr0[0] = b0 + w * 2048; sr0[1] = sr0[0] ^ 64;
    sr1[0] = (w == 6) ? e0 : b0 + (w + 4) * 2048 * (w < 6); sr1[1] = sr1[0] ^ 64;
    int f = q | ((g & 1) << 2);
#pragma unroll
    for (int T = 0; T < 4; T++) {
      int u;
      if (ffn) u = 32 * (T >> 1) + 8 * s + 4 * (T & 1) + p;
      else {
        int qb = 5 * s, kb = 20 + 5 * s, vb = 42 + 5 * s;
        if (T == 0) u = kb + p;
        else if (T == 1) u = p == 0 ? kb + 4 : p == 1 ? vb : p == 2 ? vb + 1 : vb + 3;
        else if (T == 2) u = p == 0 ? qb : p == 1 ? vb + 2 : vb + 4;
        else u = p == 0 ? qb + 4 : qb + p;
      }
      wr[T] = WB0 + (8 * g + q) * 512 + (((u >> 1) ^ (f << 1)) << 4) + (u & 1) * 8;
    }
  }
  const bool st1 = (w >= 4 && w < 7);
  f32x4 acc[6][4];
#pragma unroll
  for (int b = 0; b < 6; b++)
#pragma unroll
    for (int T = 0; T < 4; T++) acc[b][T] = (f32x4){0.f, 0.f, 0.f, 0.f};
  float s0 = 0.f, q0 = 0.f, s1 = 0.f, q1 = 0.f;

  auto loadF = [&](Frag& F, int kk) {
#pragma unroll
    for (int T = 0; T < 4; T++) F.w[T] = lds_tr2(smem + wr[T] + kk * 32 * 512, 4 * 512);
#pragma unroll
    for (int b = 0; b < 5; b++) F.x[b] = lds_b128(smem + xr[kk] + b * 2048);
  };
  auto step = [&](Frag& C, Frag& N, int kk, bool doload) {
    b16x8 sf;
    if (doload) { loadF(N, kk); sf = lds_b128(smem + sr0[kk]); }
#pragma unroll
    for (int b = 0; b < 5; b++)
#pragma unroll
      for (int T = 0; T < 4; T++) acc[b][T] = mfma(C.w[T], C.x[b], acc[b][T]);
    if (doload) stat_acc(sf, s0, q0);
    if (doload) {
#pragma unroll
      for (int i = 0; i < 14; i++) {
        __builtin_amdgcn_sched_group_barrier(0x008, 1, 0);
        __builtin_amdgcn_sched_group_barrier(0x100, 1, 0);
      }
#pragma unroll
      for (int i = 0; i < 6; i++) {
        __builtin_amdgcn_sched_group_barrier(0x008, 1, 0);
        __builtin_amdgcn_sched_group_barrier(0x002, 4, 0);
      }
    }
    if (isA) {
      if (doload) N.x[5] = lds_b128(smem + er[kk]);
#pragma unroll
      for (int T = 0; T < 4; T++) acc[5][T] = mfma(C.w[T], C.x[5], acc[5][T]);
    } else if (st1) {
      if (doload) { b16x8 sg = lds_b128(smem + sr1[kk]); stat_acc(sg, s1, q1); }
    }
  };

  Frag Fa, Fb;
  Fa.x[5] = Fb.x[5] = (b16x8)(__bf16)0.f;
  WAIT_VM(13); WAIT_LGKM0(); BAR();
  loadF(Fa, 0);
  if (isA) Fa.x[5] = lds_b128(smem + er[0]);
  { b16x8 sf = lds_b128(smem + sr0[0]); stat_acc(sf, s0, q0); if (st1) { b16x8 sg = lds_b128(smem + sr1[0]); stat_acc(sg, s1, q1); } }
  step(Fa, Fb, 1, true);
  int xo = 0, eo = 0, wo = 0;
#pragma unroll 1
  for (int i = 1; i <= 9; i++) {
    if (i == 9) WAIT_VM(0); else WAIT_VM(6);
    WAIT_LGKM0(); BAR();
    if (i <= 7) issue(xo, eo, wo, true);
    else if (i == 8 && ext0) dma16_masked(ga[0], dst[0] + eo, mask0);
    int dx = (xo == 2 * XBUF) ? -2 * XBUF : XBUF, de = eo ? -EBUF : EBUF, dw = (wo == 2 * WBUF) ? -2 * WBUF : WBUF;
    xo += dx; eo += de; wo += dw;
    int d1 = (w == 6) ? de : dx;
    xr[0] += dx; xr[1] += dx; er[0] += de; er[1] += de; sr0[0] += dx; sr0[1] += dx; sr1[0] += d1; sr1[1] += d1;
#pragma unroll
    for (int T = 0; T < 4; T++) wr[T] += dw;
    step(Fb, Fa, 0, true);
    step(Fa, Fb, 1, true);
  }
  step(Fb, Fa, 0, false);

  WAIT_LGKM0(); BAR();
  {
    auto fin = [&](float ss, float qq, int blk) {
      ss = xrow_sum(ss); qq = xrow_sum(qq);
      float m = ss * (1.f / 640.f);
      float R = fmaxf(qq * (1.f / 640.f) - m * m, 0.f) + 1e-5f;
      float rs = __builtin_amdgcn_rsqf(R);
      rs = rs * (1.5f - 0.5f * R * rs * rs);
      const float c0 = -2.f * 0.7978845608028654f * 1.4426950408889634f, c1 = c0 * 0.044715f;
      f32x4 v = ffn ? (f32x4){m, c1 * rs * rs * rs, c0 * rs, R} : (f32x4){m, rs, 0.f, 0.f};
      if (g == 0) *(LAS f32x4*)(smem + ST_OFF + (blk * 16 + c) * 16) = v;
    };
    fin(s0, q0, w);
    if (st1) fin(s1, q1, w + 4);
    if (isA && c == 4) {
#pragma unroll
      for (int T = 0; T < 4; T++) *(LAS f32x4*)(smem + CS_OFF + (s * 4 + T) * 64 + g * 16) = acc[5][T];
    }
  }
  WAIT_LGKM0(); BAR();
  f32x4 cs[4];
#pragma unroll
  for (int T = 0; T < 4; T++) cs[T] = *(LAS f32x4*)(smem + CS_OFF + (s * 4 + T) * 64 + g * 16);
  const int b0 = isA ? 0 : 5;
  if (ffn) {
    char* Hb = H + (size_t)((20 + grp * 4 + s) * 4 + g) * 2048 * 16;
#pragma unroll
    for (int b = 0; b < 6; b++) {
      if (b == 5 && !isA) break;
      int blk = b == 5 ? 10 : b0 + b;
      f32x4 st = *(LAS f32x4*)(smem + ST_OFF + (blk * 16 + c) * 16);
      float h[8];
#pragma unroll
      for (int t = 0; t < 2; t++)
#pragma unroll
        for (int j = 0; j < 4; j++) {
          float gg = fmaf(-st[0], cs[t][j], acc[b][t][j]);
          float uu = fmaf(-st[0], cs[2 + t][j], acc[b][2 + t][j]);
          float pp = fmaf(gg * gg, st[1], st[2]);
          float e = __builtin_amdgcn_exp2f(gg * pp);
          float r = __builtin_amdgcn_rcpf(fmaf(e, st[3], st[3]));
          h[4 * t + j] = gg * uu * r;
        }
      u32x4 o = {pk2(h[0], h[1]), pk2(h[2], h[3]), pk2(h[4], h[5]), pk2(h[6], h[7])};
      int slot = b == 5 ? t0 + 155 + c : t0 + 16 * blk + c;
      bool ok = slot < tend && (b != 5 || c >= 5);
      if (ok) *(u32x4*)(Hb + (size_t)slot * 16) = o;
    }
  } else {
    u32x4 Kf[6], Qf[5];
    float cq[5];
    const int vq1 = g == 0 ? 5 : g == 3 ? 3 : g - 1, vq2 = g == 1 ? 2 : g == 2 ? 4 : 5;
#pragma unroll
    for (int b = 0; b < 6; b++) {
      if (b == 5 && !isA) break;
      int blk = b == 5 ? 10 : b0 + b;
      int vb = b == 5 ? 0 : b0 + b + 1;
      f32x2 st = *(LAS f32x2*)(smem + ST_OFF + (blk * 16 + c) * 16);
      float y[4][4];
      float f2 = g == 0 ? 1.f : st[1];
#pragma unroll
      for (int j = 0; j < 4; j++) {
        y[0][j] = fmaf(-st[0], cs[0][j], acc[b][0][j]) * st[1];
        y[1][j] = fmaf(-st[0], cs[1][j], acc[b][1][j]) * st[1];
        y[2][j] = fmaf(-st[0], cs[2][j], acc[b][2][j]) * f2;
        y[3][j] = fmaf(-st[0], cs[3][j], acc[b][3][j]);
      }
      u32x2 P[4];
#pragma unroll
      for (int T = 0; T < 4; T++) P[T] = (u32x2){pk2(y[T][0], y[T][1]), pk2(y[T][2], y[T][3])};
      LAS char* vrow = smem + V_OFF + s * (176 * 48) + (vb * 16 + c) * 48;
      *(LAS u32x2*)(vrow + vq1 * 8) = P[1];
      *(LAS u32x2*)(vrow + vq2 * 8) = P[2];
      Kf[b] = (u32x4){P[0][0], P[0][1], g == 0 ? P[1][0] : 0u, g == 0 ? P[1][1] : 0u};
      if (b < 5) {
        Qf[b] = (u32x4){g == 0 ? P[2][0] : P[3][0], g == 0 ? P[2][1] : P[3][1], P[3][0], P[3][1]};
        cq[b] = st[1] * (0.22360679774997896f * 1.4426950408889634f);
      }
    }
    if (isA) *(LAS u32x4*)(smem + KX_OFF + s * 1024 + l * 16) = Kf[4];
    WAIT_LGKM0(); BAR();
    if (!isA) Kf[5] = *(LAS u32x4*)(smem + KX_OFF + s * 1024 + l * 16);
    f32x4 Cc, Cp;
#pragma unroll
    for (int j = 0; j < 4; j++) {
      int kk = 4 * g + j;
      Cc[j] = (kk <= c && kk >= c - 7) ? 0.f : -1e30f;
      Cp[j] = (kk >= c + 9) ? 0.f : -1e30f;
    }
    const bool usePrev = 4 * g + 3 >= c + 9;
    LAS char* vbase = smem + V_OFF + s * (176 * 48) + (b0 * 16 + 4 * g + q) * 48;
    LAS char* obase = smem + O_OFF + (b0 * 16 + c) * 16;
    const int Q0 = 5 * s + g, Q1 = 5 * s + 4;
#pragma unroll
    for (int b = 0; b < 5; b++) {
      int slot0 = t0 + 16 * (b0 + b);
      bool start = (slot0 & 1023) == 0;
      f32x4 cp = Cp;
      if (start) cp = (f32x4){-1e30f, -1e30f, -1e30f, -1e30f};
      b16x8 kp = __builtin_bit_cast(b16x8, b == 0 ? Kf[5] : Kf[b - 1]);
      f32x4 Sp = mfma(kp, __builtin_bit_cast(b16x8, Qf[b]), cp);
      f32x4 Sc = mfma(__builtin_bit_cast(b16x8, Kf[b]), __builtin_bit_cast(b16x8, Qf[b]), Cc);
      float sv[4];
#pragma unroll
      for (int j = 0; j < 4; j++) sv[j] = fmaxf(Sp[j], Sc[j]);
      float m = xrow_max(fmaxf(fmaxf(sv[0], sv[1]), fmaxf(sv[2], sv[3])));
      float mc = -m * cq[b];
      float pv[4];
#pragma unroll
      for (int j = 0; j < 4; j++) pv[j] = __builtin_amdgcn_exp2f(fmaf(sv[j], cq[b], mc));
      float r = __builtin_amdgcn_rcpf(xrow_sum((pv[0] + pv[1]) + (pv[2] + pv[3])));
      unsigned p0 = pk2(pv[0] * r, pv[1] * r), p1 = pk2(pv[2] * r, pv[3] * r);
      u32x4 Pf = {usePrev ? p0 : 0u, usePrev ? p1 : 0u, usePrev ? 0u : p0, usePrev ? 0u : p1};
      b16x8 V0 = lds_tr2(vbase + b * 16 * 48 + p * 8, 16 * 48);
      b16x8 V1 = lds_tr2(vbase + b * 16 * 48 + 32, 16 * 48);
      f32x4 z = {0.f, 0.f, 0.f, 0.f};
      f32x4 O0 = mfma(V0, __builtin_bit_cast(b16x8, Pf), z);
      f32x4 O1 = mfma(V1, __builtin_bit_cast(b16x8, Pf), z);
      *(LAS u32x2*)(obase + b * 256 + (Q0 >> 1) * 2560 + (Q0 & 1) * 8) = (u32x2){pk2(O0[0], O0[1]), pk2(O0[2], O0[3])};
      if (g == 0) *(LAS u32x2*)(obase + b * 256 + (Q1 >> 1) * 2560 + (Q1 & 1) * 8) = (u32x2){pk2(O1[0], O1[1]), pk2(O1[2], O1[3])};
    }
    WAIT_LGKM0(); BAR();
#pragma unroll
    for (int i = 0; i < 4; i++) {
      int n = tid + 512 * i;
      if (n < 1600) {
        int kg = n / 160, sl = n - kg * 160;
        u32x4 v = *(LAS u32x4*)(smem + O_OFF + n * 16);
        if (t0 + sl < tend) *(u32x4*)(H + ((size_t)(grp * 10 + kg) * 2048 + t0 + sl) * 16) = v;
      }
    }
  }
}

__global__ __launch_bounds__(512, 2) void k_phase2(const char* __restrict__ X, const char* __restrict__ Wo, const char* __restrict__ Wd,
                                                   const char* __restrict__ H, char* __restrict__ Out) {
  __shared__ __attribute__((aligned(16))) char smem_[LDS_BYTES];
  LAS char* smem = (LAS char*)smem_;
  const int tid = threadIdx.x, l = tid & 63;
  const int w = __builtin_amdgcn_readfirstlane(tid >> 6), s = w & 3;
  const bool isA = w < 4;
  const int g = l >> 4, c = l & 15, q = c >> 2, p = l & 3;
  const int bid = blockIdx.x, xj = bid & 7, s32 = bid >> 3;
  const int tt = xj * 4 + (s32 >> 3), ct = s32 & 7;
  const int ks0 = 17 * s + (isA ? 0 : 9);
  int offo[5], offh[5];
#pragma unroll
  for (int i = 0; i < 5; i++) {
    int n = 64 * i + l, rho = n / 10, ch = n - rho * 10;
    int qq = rho & 3, gg = ((rho >> 2) & 1) | ((rho >> 4) << 1), tt2 = (rho >> 3) & 1;
    offo[i] = (8 * gg + 4 * tt2 + qq) * 1280 + ct * 160 + ch * 16;
    offh[i] = (16 * tt2 + 4 * gg + qq) * 1280 + ct * 160 + ch * 16;
  }
  auto wdma = [&](int ks, int slot) {
    const bool o = ks < 20;
    const char* base = o ? Wo + (size_t)ks * 32 * 1280 : Wd + (size_t)(ks - 20) * 32 * 1280;
#pragma unroll
    for (int i = 0; i < 5; i++) dma16(base + (o ? offo[i] : offh[i]), w * 20480 + slot * 5120 + i * 1024);
  };
  LAS char* wrd = smem + w * 20480 + (q + 4 * (g & 1) + 16 * (g >> 1)) * 160 + p * 8;
  b16x8 Wr[5][5];
#pragma unroll
  for (int j = 0; j < 4; j++) wdma(ks0 + j, j);
  WAIT_VM(0);
#pragma unroll
  for (int j = 0; j < 4; j++)
#pragma unroll
    for (int T = 0; T < 5; T++) Wr[j][T] = lds_tr2(wrd + j * 5120 + T * 32, 8 * 160);
  WAIT_LGKM0();
  if (isA) {
    wdma(ks0 + 8, 0);
    WAIT_VM(0);
#pragma unroll
    for (int T = 0; T < 5; T++) Wr[4][T] = lds_tr2(wrd + T * 32, 8 * 160);
    WAIT_LGKM0();
  } else {
#pragma unroll
    for (int T = 0; T < 5; T++) Wr[4][T] = (b16x8)(__bf16)0.f;
  }
#pragma unroll
  for (int j = 0; j < 4; j++) wdma(ks0 + 4 + j, j);
  WAIT_VM(0);
  const char* hp = H + ((size_t)(ks0 * 4 + g) * 2048 + tt * 64 + c) * 16;
  f32x4 acc[4][5];
#pragma unroll
  for (int b = 0; b < 4; b++)
#pragma unroll
    for (int T = 0; T < 5; T++) acc[b][T] = (f32x4){0.f, 0.f, 0.f, 0.f};
  b16x8 hf[9][4];
#pragma unroll
  for (int j = 0; j < 8; j++)
#pragma unroll
    for (int b = 0; b < 4; b++) hf[j][b] = *(const b16x8*)(hp + (size_t)j * 4 * 2048 * 16 + b * 256);
#pragma unroll
  for (int j = 0; j < 8; j++) {
    b16x8 wf[5];
#pragma unroll
    for (int T = 0; T < 5; T++) wf[T] = j < 4 ? Wr[j][T] : lds_tr2(wrd + (j - 4) * 5120 + T * 32, 8 * 160);
#pragma unroll
    for (int b = 0; b < 4; b++)
#pragma unroll
      for (int T = 0; T < 5; T++) acc[b][T] = mfma(wf[T], hf[j][b], acc[b][T]);
  }
  if (isA) {
#pragma unroll
    for (int b = 0; b < 4; b++) hf[8][b] = *(const b16x8*)(hp + (size_t)8 * 4 * 2048 * 16 + b * 256);
#pragma unroll
    for (int b = 0; b < 4; b++)
#pragma unroll
      for (int T = 0; T < 5; T++) acc[b][T] = mfma(Wr[4][T], hf[8][b], acc[b][T]);
  }
  WAIT_LGKM0(); BAR();
#pragma unroll
  for (int b = 0; b < 4; b++)
#pragma unroll
    for (int T = 0; T < 5; T++) *(LAS f32x4*)(smem + w * 20480 + (b * 5 + T) * 1024 + l * 16) = acc[b][T];
  WAIT_LGKM0(); BAR();
#pragma unroll
  for (int i = 0; i < 3; i++) {
    int tile = w + 8 * i;
    if (tile < 20) {
      int b = tile / 5, T = tile - b * 5;
      f32x4 a = *(LAS f32x4*)(smem + tile * 1024 + l * 16);
#pragma unroll
      for (int v = 1; v < 8; v++) a += *(LAS f32x4*)(smem + v * 20480 + tile * 1024 + l * 16);
      size_t off = (size_t)tok_of_slot(tt * 64 + b * 16 + c) * 1280 + (ct * 80 + T * 16 + g * 4) * 2;
      u32x2 xv = *(const u32x2*)(X + off);
      a[0] += __uint_as_float(xv[0] << 16); a[1] += __uint_as_float(xv[0] & 0xffff0000u);
      a[2] += __uint_as_float(xv[1] << 16); a[3] += __uint_as_float(xv[1] & 0xffff0000u);
      *(u32x2*)(Out + off) = (u32x2){pk2(a[0], a[1]), pk2(a[2], a[3])};
    }
  }
}

extern "C" void kernel_launch(void* const* d_in, const int* in_sizes, int n_in, void* d_out, int out_size, void* d_ws, size_t ws_size,
                              hipStream_t stream) {
  const char* x = (const char*)d_in[0];
  k_phase1<<<256, 512, 0, stream>>>(x, (const char*)d_in[1], (const char*)d_in[4], (const char*)d_in[5], (char*)d_ws);
  k_phase2<<<256, 512, 0, stream>>>(x, (const char*)d_in[2], (const char*)d_in[6], (const char*)d_ws, (char*)d_out);
}
```

```cpp
#define OUTAUX 2
#include <hip/hip_runtime.h>
#include <stdint.h>
#include <type_traits>

#define LAS __attribute__((address_space(3)))
#define DI __device__ __forceinline__
typedef short s16x4 __attribute__((ext_vector_type(4)));
typedef __bf16 b16x2 __attribute__((ext_vector_type(2)));
typedef __bf16 b16x8 __attribute__((ext_vector_type(8)));
typedef float f32x2 __attribute__((ext_vector_type(2)));
typedef float f32x4 __attribute__((ext_vector_type(4)));
typedef unsigned u32x2 __attribute__((ext_vector_type(2)));
typedef unsigned u32x4 __attribute__((ext_vector_type(4)));
typedef unsigned long long u64;

constexpr int LDS_BYTES = 163840;
constexpr int XBUF = 20480, EB0 = 61440, EBUF = 1536, JUNK = 64512, WB0 = 65536, WBUF = 16384, WHALF = 49152;
constexpr int V_OFF0 = 61440, V_OFF1 = 112640, V_HEAD = 176 * 48;
constexpr int CS_OFF = V_OFF0 + 2 * V_HEAD + 2048;
constexpr int O_OFF0 = V_OFF0, O_OFF1 = V_OFF1;
constexpr int WS_E = 4096, WS_FF = 8192, WS_FA = 12288, WS_ST = 16384, WS_W2 = 65536, WS_H = 4194304, WS_BYTES = WS_H + 272 * 2048 * 16;
constexpr int OOB = 0x7FFFFFF0;
constexpr unsigned SPIN_CAP = 1u << 16;
#define CODE_LINES 12

template <int I, int N, class F> DI void sfor(F&& f) {
  if constexpr (I < N) { f(std::integral_constant<int, I>{}); sfor<I + 1, N>(f); }
}
DI unsigned pk2(float a, float b) { f32x2 v = {a, b}; return __builtin_bit_cast(unsigned, __builtin_convertvector(v, b16x2)); }
DI unsigned pk2(f32x2 v) { return __builtin_bit_cast(unsigned, __builtin_convertvector(v, b16x2)); }
DI f32x2 lo2(f32x4 v) { return __builtin_shufflevector(v, v, 0, 1); }
DI f32x2 hi2(f32x4 v) { return __builtin_shufflevector(v, v, 2, 3); }
DI f32x2 bc2(float v) { return (f32x2){v, v}; }
DI f32x2 fma2(f32x2 a, f32x2 b, f32x2 c) { return __builtin_elementwise_fma(a, b, c); }
DI f32x4 mfma(b16x8 a, b16x8 b, f32x4 c) { return __builtin_amdgcn_mfma_f32_16x16x32_bf16(a, b, c, 0, 0, 0); }
DI f32x4 mfma(u32x4 a, u32x4 b, f32x4 c) { return mfma(__builtin_bit_cast(b16x8, a), __builtin_bit_cast(b16x8, b), c); }
DI void mfma_acc(f32x4& c, b16x8 a, b16x8 b) { asm volatile("v_mfma_f32_16x16x32_bf16 %0, %1, %2, %0" : "+v"(c) : "v"(a), "v"(b)); }
DI b16x8 lds_b128(LAS char* p) { return *(LAS b16x8*)p; }
DI s16x4 lds_tr(LAS char* p) { return __builtin_amdgcn_ds_read_tr16_b64_v4i16((LAS s16x4*)p); }
DI b16x8 cat(s16x4 a, s16x4 b) { return __builtin_bit_cast(b16x8, __builtin_shufflevector(a, b, 0, 1, 2, 3, 4, 5, 6, 7)); }
DI b16x8 lds_tr2(LAS char* p, int d) { return cat(lds_tr(p), lds_tr(p + d)); }
DI void dmas(unsigned voff, const char* sb, unsigned dst) {
  asm volatile("s_mov_b32 m0, %2\n\ts_nop 0\n\tglobal_load_lds_dwordx4 %0, %1" ::"v"(voff), "s"(sb), "s"(dst) : "memory");
}
DI void dmas_sc1(unsigned voff, const char* sb, unsigned dst) {
  asm volatile("s_mov_b32 m0, %2\n\ts_nop 0\n\tglobal_load_lds_dwordx4 %0, %1 sc1" ::"v"(voff), "s"(sb), "s"(dst) : "memory");
}
DI void dmas_masked(unsigned voff, const char* sb, unsigned dst, u64 mask) {
  asm volatile("s_mov_b32 m0, %2\n\ts_mov_b64 exec, %3\n\tglobal_load_lds_dwordx4 %0, %1\n\ts_mov_b64 exec, -1" ::"v"(voff), "s"(sb), "s"(dst), "s"(mask) : "memory");
}
DI void touch(const char* a) { asm volatile("s_mov_b32 m0, %1\n\ts_nop 0\n\tglobal_load_lds_dword %0, off" ::"v"(a), "n"(JUNK) : "memory"); }
template <class R> DI void st128(u32x4 v, R rsrc, int voff, int soff) {
  __builtin_amdgcn_raw_buffer_store_b128(v, rsrc, voff, soff, 16);
  asm volatile("s_nop 1" ::"v"(v) : "memory");
}
template <int N> DI void wait_vm() { asm volatile("s_waitcnt vmcnt(%0)" ::"n"(N) : "memory"); }
#define WAIT_LGKM0() do { asm volatile("" ::: "memory"); __builtin_amdgcn_s_waitcnt(0xC07F); asm volatile("" ::: "memory"); } while (0)
#define BAR() do { asm volatile("" ::: "memory"); __builtin_amdgcn_s_barrier(); asm volatile("" ::: "memory"); } while (0)
#define SB() __builtin_amdgcn_sched_barrier(0)
DI float fmx(float a, float b) { return __builtin_elementwise_maximum(a, b); }
DI int row_off(int slot) { return slot * 2560 - (slot >> 10) * (2047 * 1280); }
DI float xrow_sum(float v) {
  unsigned u = __float_as_uint(v);
  auto r = __builtin_amdgcn_permlane16_swap(u, u, false, false);
  v = __uint_as_float(r[0]) + __uint_as_float(r[1]);
  u = __float_as_uint(v);
  auto r2 = __builtin_amdgcn_permlane32_swap(u, u, false, false);
  return __uint_as_float(r2[0]) + __uint_as_float(r2[1]);
}
template <int C> DI float dpp_add(float v) { return v + __uint_as_float(__builtin_amdgcn_update_dpp(0, __float_as_uint(v), C, 0xf, 0xf, true)); }
DI float wave_sum(float v) { return xrow_sum(dpp_add<0x121>(dpp_add<0x122>(dpp_add<0x124>(dpp_add<0x128>(v))))); }

#define ST(set, k)

struct Frag { b16x8 x[6]; s16x4 w[4][2]; };
struct Roles { unsigned r[256]; };

template <bool A>
DI void body(const char* __restrict__ X, const char* __restrict__ Wqkv, const char* __restrict__ Wo, const char* __restrict__ Wg,
             const char* __restrict__ Wu, const char* __restrict__ Wd, char* __restrict__ Out, char* __restrict__ WS, const unsigned role_w,
             LAS char* smem, const int w, const char* code
) {
  constexpr int NB = A ? 6 : 5, B0 = A ? 0 : 5, NS = A ? 9 : 8;
  int tid = threadIdx.x, l = tid & 63;
  const int s = w & 3;
  int g = l >> 4, c = l & 15, q = c >> 2, p = l & 3;
  auto relane = [&]() { tid = threadIdx.x; asm volatile("" : "+v"(tid)); l = tid & 63; g = l >> 4; c = l & 15; q = c >> 2; p = l & 3; };
  const int bid = blockIdx.x, xj = bid & 7, s32 = bid >> 3;
  const bool idle = s32 == 31, ffn = s32 < 18;
  const auto wsr = __builtin_amdgcn_make_buffer_rsrc(WS, 0, WS_BYTES, 0x00020000);
  const int t0 = role_w & 2047, tend = t0 + ((role_w >> 11) & 255), grp = (role_w >> 19) & 15, tile = role_w >> 23;

  auto tx_addr = [&]() { return X + row_off(min((xj & 1) * 944 + s32 * 35 + min(l, 34), 2047)); };
  auto tw_addr = [&](int& stride64) {
    const int m = s32 * 42 + min(l, 41), ma = m - 768, ra = (ma * 7282) >> 16, r9 = ma - 9 * ra, mt = (r9 * 11) >> 5;
    const char* f = ((m & 2) ? Wu : Wg) + ((m & 255) >> 2) * 3072 + (3 * (xj >> 1) + (m >> 8)) * 256 + (m & 1) * 128;
    const char* a = Wqkv + ra * 3840 + mt * 1280 + ((5 * (xj >> 1)) >> 1) * 128 + (r9 - 3 * mt) * 128;
    stride64 = m < 768 ? 64 * 3072 : 64 * 3840;
    return m < 768 ? f : a;
  };
#define STRICT1 0
#define TMODE 2
  auto touch1 = [&](int c0) {
    const char* ta;
    if (w & 1) { int st; ta = tw_addr(st); ta += ((w >> 1) + c0) * st; }
    else ta = tx_addr() + ((w >> 1) + c0) * 128;
    touch(ta);
  };
  if (CODE_LINES && A) { if (w == 0) touch(code + (s32 * CODE_LINES + min(l, CODE_LINES - 1)) * 128); }
  if (TMODE == 1) touch1(0);
  auto row_issue = [&](u32x4& sa, unsigned& sb1) {
    asm volatile("global_load_dwordx4 %0, %2, %4\n\tglobal_load_dword %1, %3, %4 offset:1024" : "=&v"(sa), "=&v"(sb1) : "v"(l * 16), "v"(l * 4), "s"(X + row_off(8 * bid + w)) : "memory");
    SB();
  };
  auto row_stats = [&](u32x4 sa, unsigned sb1) {
    float ss = 0.f, qq = 0.f;
#pragma unroll
    for (int i = 0; i < 5; i++) {
      const unsigned d = i < 4 ? sa[i] : sb1;
      const float lo = __uint_as_float(d << 16), hi = __uint_as_float(d & 0xffff0000u);
      ss += lo + hi; qq = fmaf(lo, lo, fmaf(hi, hi, qq));
    }
    ss = wave_sum(ss); qq = wave_sum(qq);
    const float m = ss * (1.f / 640.f);
    const float R = fmaxf(qq * (1.f / 640.f) - m * m, 0.f) + 1e-5f;
    return (u32x2){__float_as_uint(m), __float_as_uint(R)};
  };
  auto st_store = [&](u32x2 v) { __builtin_amdgcn_raw_buffer_store_b64(v, wsr, l == 0 ? 0 : OOB, WS_ST + (8 * bid + w) * 8, 16); };
  u32x2 tagv;
  auto get_tag = [&]() {
    const char* p0 = WS;
    asm volatile("" : "+s"(p0));
    const u64 tag = *(const __attribute__((address_space(4))) u64*)p0 + 0x9E3779B97F4A7C15ull;
    tagv = (u32x2){(unsigned)tag, (unsigned)(tag >> 32)};
  };
  auto u_dma = [&](int f, unsigned dst) {
    const int rho = l >> 1, qq = rho & 3, gg = ((rho >> 2) & 1) | ((rho >> 4) << 1), t2 = (rho >> 3) & 1;
    const int ks = f / 40, c16 = f - ks * 40;
    const char* base = (ks < 20 ? Wo + ks * 40960 : Wd + (ks - 20) * 40960) + c16 * 32;
    dmas(((ks < 20 ? 8 * gg + 4 * t2 : 16 * t2 + 4 * gg) + qq) * 1280 + (l & 1) * 16, base, dst);
  };
  auto u_read = [&](unsigned src) { return __builtin_bit_cast(u32x4, lds_tr2(smem + src + (q + 4 * (g & 1) + 16 * (g >> 1)) * 32 + p * 8, 256)); };
  auto u_store = [&](int f, u32x4 v) {
    const int ks = f / 40, c16 = f - ks * 40, c5 = c16 / 5;
    st128(v, wsr, l * 16, WS_W2 + ((c5 * 68 + ks) * 5 + (c16 - c5 * 5)) * 1024);
  };
  u32x4 e0, e1;
  auto e_issue = [&]() {
    asm volatile("global_load_dwordx4 %0, %2, %3 sc1\n\tglobal_load_dwordx4 %1, %2, %3 offset:16 sc1" : "=&v"(e0), "=&v"(e1) : "v"(l * 32), "s"(WS + WS_E) : "memory");
  };
  auto e_spin = [&]() {
#pragma unroll 1
    for (unsigned n = 0; n < SPIN_CAP; n++) {
      const unsigned d = ((e0[0] ^ tagv[0]) | (e0[1] ^ tagv[1]) | (e0[2] ^ tagv[0]) | (e0[3] ^ tagv[1])) | ((e1[0] ^ tagv[0]) | (e1[1] ^ tagv[1]) | (e1[2] ^ tagv[0]) | (e1[3] ^ tagv[1]));
      if (__builtin_amdgcn_ballot_w64(d != 0) == 0) break;
      __builtin_amdgcn_s_sleep(2);
      e_issue();
      asm volatile("s_waitcnt vmcnt(0)" : "+v"(e0), "+v"(e1)::"memory");
    }
  };
  int tt, ct, ks0, slotL0, slotL1, slotL2, slotL3;
  auto geo = [&]() {
    int s_ = s, b_ = bid;
    asm volatile("" : "+s"(s_), "+s"(b_));
    tt = (b_ & 7) * 4 + (b_ >> 6); ct = (b_ >> 3) & 7;
    ks0 = 17 * s_ + (A ? 0 : 9);
    slotL0 = (A ? 4 + s_ : (s_ < 3 ? 16 + s_ : 26)) * 5120; slotL1 = (A ? 8 + s_ : (s_ < 3 ? 19 + s_ : 27)) * 5120;
    slotL2 = (A ? s_ : 28 + s_) * 5120; slotL3 = (A ? 12 + s_ : 22 + s_) * 5120;
  };
#define ROT 0
  auto ks_of = [&](int j) {
    if (ROT == 0) return ks0 + j;
    const int k = j + 2 * (tt & 3);
    return ks0 + (k >= NS ? k - NS : k);
  };
  auto w2_dma = [&](int j, int i, int slot) { dmas_sc1(l * 16, WS + WS_W2 + ((ct * 68 + ks_of(j)) * 5 + i) * 1024, slot + i * 1024); };
  u32x4 Wr[5][5];
  auto w2_reg1 = [&](int n) { Wr[n / 5][n % 5] = __builtin_amdgcn_raw_buffer_load_b128(wsr, l * 16 + (n % 5) * 1024, WS_W2 + (ct * 68 + ks_of(n < 20 ? n / 5 : NS - 1)) * 5120, 16); };

  if (idle) {
    const int f1 = 2048 + xj * 84 + w;
    relane();
    if (TMODE == 2) touch1(1);
    u32x4 sa; unsigned sb1;
    row_issue(sa, sb1);
    get_tag(); geo();
    u_dma(bid * 8 + w, (65536 + w * 12288));
#pragma unroll 1
    for (int i = 0; i < (A ? 11 : 10); i++) u_dma(f1 + 8 * i, (65536 + w * 12288) + (i + 1) * 1024);
    asm volatile("s_waitcnt vmcnt(0)" : "+v"(sa), "+v"(sb1)::"memory");
    st_store(row_stats(sa, sb1));
    u_store(bid * 8 + w, u_read((65536 + w * 12288)));
#pragma unroll 1
    for (int i = 0; i < (A ? 11 : 10); i++) u_store(f1 + 8 * i, u_read((65536 + w * 12288) + (i + 1) * 1024));
    wait_vm<0>(); WAIT_LGKM0(); BAR();
    if (!A && w == 7) {
      __builtin_amdgcn_raw_buffer_store_b64(tagv, wsr, l == 0 ? 0 : OOB, WS_E + bid * 8, 16);
      e_issue();
      asm volatile("s_waitcnt vmcnt(0)" : "+v"(e0), "+v"(e1)::"memory");
      e_spin();
      if (xj == 0) __builtin_amdgcn_raw_buffer_store_b64(tagv, wsr, l == 0 ? 0 : OOB, 0, 16);
    }
    BAR();
#pragma unroll 1
    for (int i = 0; i < 5; i++) { w2_dma(4, i, slotL0); w2_dma(5, i, slotL1); w2_dma(6, i, slotL2); }
#pragma unroll
    for (int n = 0; n < 25; n++) w2_reg1(n);
  } else {
    u32x4 sa; unsigned sb1;
    row_issue(sa, sb1);
    unsigned vx[3], vw[2], dstx[3];
    const bool ext0 = !A && w >= 6;
    const int wstride = ffn ? 3072 : 3840;
    const u64 mask0 = (!A && w == 6) ? 0xFFFFFF0000000000ull : ~0ull;
    const unsigned vx0 = (l >> 3) * 2560 + (((l & 7) ^ g) << 4);
#pragma unroll
    for (int i = 0; i < 3; i++) {
      const int w3 = 3 * w + i, w14 = 14 + w, eadj = ffn ? -5 : -176;
      const int pc = A ? w3 : i == 0 ? (ext0 ? w14 : w3) : min(w3, 17 + i);
      int s0 = t0 + 8 * pc + (i == 0 && ext0 ? eadj : 0);
      if (!A) asm("s_max_i32 %0, %0, 0\n\ts_min_i32 %0, %0, 0x7f8" : "+s"(s0)::"scc");
      vx[i] = (vx0 ^ ((pc & 1) << 6)) + row_off(s0);
      dstx[i] = pc * 1024 + (i == 0 && ext0 ? EB0 - 512 - 20 * 1024 : 0);
      if (i == 0 && !A) dmas_masked(vx[0], X, dstx[0], mask0); else dmas(vx[i], X, dstx[i]);
      SB();
    }
#pragma unroll
    for (int i = 0; i < 2; i++) {
      const int lc = c ^ (g << 1) ^ (i << 3), la = lc + (A ? 0 : 16);
      const int m1 = la >= 10, m2 = la >= 21;
      const int ca = (m1 + m2) * 1280 + min(la - 10 * m1 - 11 * m2, 9) * 16, cf = lc * 16;
      vw[i] = g * wstride + (ffn ? cf : ca);
    }
    const char* wb = (ffn ? (A ? Wg : Wu) : Wqkv) + (unsigned)(grp * (ffn ? 256 : 160) + 16 * s * wstride);
    const unsigned dstw = WB0 + (A ? 0 : WHALF) + s * 4096;
    auto dslot = [&](int i, int ch, int che) {
      if (i == 0 && !A) dmas_masked(vx[0], X + (ext0 ? che : ch) * 128, dstx[0] + (ext0 ? (che & 1) * EBUF : (ch % 3) * XBUF), mask0);
      else if (i < 3) dmas(vx[i], X + ch * 128, dstx[i] + (ch % 3) * XBUF);
      else dmas(vw[(i - 3) >> 1], wb + (ch * 64 + (i - 3) * 4) * wstride, dstw + (i - 3) * 1024 + (ch % 3) * WBUF);
    };
    ST(2, 1);
#pragma unroll
    for (int i = 3; i < 7; i++) dslot(i, 0, 0);
    ST(2, 2);
    const unsigned ust = dstw + 3 * 1024 + 2 * WBUF;
    u_dma(bid * 8 + w, ust);
    if (TMODE == 2) touch1(1);
    get_tag(); geo();
    *(LAS unsigned*)(smem + EB0 + (l >> 5) * EBUF + (l & 31) * 4) = 0x3F803F80u;
    int xr[2], er[2], wr[4];
    {
      const int re = max(c, 4);
      xr[0] = c * 128 + ((g ^ (c >> 1)) << 4) + B0 * 2048; xr[1] = xr[0] ^ 64;
      er[0] = EB0 + (re - 4) * 128 + ((g ^ (re >> 1)) << 4); er[1] = er[0] ^ 64;
      const int f = q | ((g & 1) << 2);
#pragma unroll
      for (int T = 0; T < 4; T++) {
        const unsigned map = T == 0 ? 0x17161514u : T == 1 ? 0x2D2B2A18u : T == 2 ? 0x2E2E2C00u : 0x03020104u;
        const int ua = 5 * s + ((map >> (8 * p)) & 255), uf = 32 * (T >> 1) + 8 * s + 4 * (T & 1) + p;
        const int u = ffn ? uf : ua;
        wr[T] = WB0 + (u >> 5) * WHALF + (8 * g + q) * 256 + ((((u >> 1) & 15) ^ (f << 1)) << 4) + (u & 1) * 8;
      }
    }
    const char* tpx = X; const char* tpw = X; int tst = 0;
    if constexpr (!A) { tpx = tx_addr(); tpw = tw_addr(tst); }
    auto dslot7 = [&](int n, int ch, int che, int tch) {
      const int tc = tch + (TMODE == 2);
      if (TMODE && !A && tc < 10 && w == 7) { if (n == 1) touch(tpx + tc * 128); else touch(tpw + tc * tst); }
      else dslot(n, ch, che);
    };
    ST(2, 3);
    asm volatile("s_waitcnt vmcnt(%2)" : "+v"(sa), "+v"(sb1) : "n"(8 + (TMODE == 2) - STRICT1) : "memory");
    ST(2, 4);
    u32x2 mystat = row_stats(sa, sb1);
    asm volatile("" : "+v"(mystat));
    u32x4 myfrag;
    ST(2, 5);
#pragma unroll
    for (int i = 0; i < 3; i++) dslot(i, 1, 1);
    f32x4 acc[NB][4];
#pragma unroll
    for (int b = 0; b < NB; b++)
#pragma unroll
      for (int T = 0; T < 4; T++) acc[b][T] = (f32x4){0.f, 0.f, 0.f, 0.f};
#pragma unroll
    for (int b = 0; b < NB; b++) asm volatile("" : "+v"(acc[b][0]), "+v"(acc[b][1]), "+v"(acc[b][2]), "+v"(acc[b][3]));
    u32x2 stv[NB];
    constexpr int NEARLY = A ? 10 : 15;

    {
      constexpr int NM = 4 * NB, NR = 8 + NB;
      Frag Fa, Fb;
      auto rd = [&](Frag& F, int ch, int kk, int n) {
        if (n < 8) F.w[n >> 1][n & 1] = lds_tr(smem + wr[n >> 1] + (ch % 3) * WBUF + (kk * 32 + (n & 1) * 4) * 256);
        else if (n < 13) F.x[n - 8] = lds_b128(smem + xr[kk] + (ch % 3) * XBUF + (n - 8) * 2048);
        else F.x[5] = lds_b128(smem + er[kk] + (ch & 1) * EBUF);
      };
      auto step = [&](Frag& C, Frag& N, int ch, int kk, bool doload, int gap, auto&& mem) {
#pragma unroll
        for (int m = 0; m < NM; m++) {
          if (m == 0) { if (kk == 0) __builtin_amdgcn_s_setprio(3); else __builtin_amdgcn_s_setprio(1); }
          if (m == NM / 2) { if (kk == 0) __builtin_amdgcn_s_setprio(2); else __builtin_amdgcn_s_setprio(0); }
          mfma_acc(acc[m >> 2][m & 3], cat(C.w[m & 3][0], C.w[m & 3][1]), C.x[m >> 2]);
          if (doload && m < NR) rd(N, ch, kk, m);
          if (m % gap == gap - 2) mem(m / gap);
          SB();
        }
      };
      ST(2, 6);
      wait_vm<4 + (TMODE == 2)>(); ST(2, 7);
      WAIT_LGKM0(); BAR();
      ST(0, 1); ST(1, 1); ST(2, 8);
#pragma unroll
      for (int n = 0; n < NR; n++) rd(Fa, 0, 0, n);
      SB();
      ST(2, 9);
      step(Fa, Fb, 0, 1, true, A ? 3 : 2, [&](int n) {
        if (n < 4) dslot(n + 3, 1, 1);
        else if (n == 4) { if (!ext0) dslot(0, 2, 0); else dslot(1, 2, 0); }
        else if (n < 7) dslot7(n - 4, 2, 0, 4);
      });
      sfor<1, 10>([&](auto ic) {
        constexpr int i = decltype(ic)::value;
        if (i == 1) ST(2, 10);
        if constexpr (i == 5 && !A) { if (w == 7) wait_vm<7>(); else wait_vm<6>(); }
        else wait_vm<i == 1 ? 3 : i == 3 ? 8 : i == 9 ? (A ? 10 : 12) : 6>();
        if (i == 1) ST(2, 11);
        if constexpr (i == 8 && !A) {
          if (w == 7) { asm volatile("s_waitcnt vmcnt(4)" : "+v"(e0), "+v"(e1)::"memory"); e_spin(); }
        }
        WAIT_LGKM0(); BAR();
        ST(1, 1 + i);
        if (i == 5) ST(0, 2);
        if constexpr (i == 1) { myfrag = u_read(ust); SB(); }
        auto mem = [&](int n) {
          if (i == 1) {
            if (n == 0) dslot(0, 3, 2);
            else if (n < 5) { if (n == 4) asm volatile("" : "+v"(myfrag)); dslot(n + 2, 2, 0); }
            else if (n < 7) dslot7(n - 4, 3, 2, 5);
            else if (n < 11) dslot(n - 4, 3, 2);
          } else if (i <= 7) {
            if (n >= 7) return;
            if (n == 1 || n == 2) {
              if (i <= 5) dslot7(n, i + 2, i + 1, i + 4);
              else if (!A && i == 7) { if (w == 7) { if (n == 1) e_issue(); } else dslot(n, i + 2, i + 1); }
              else dslot(n, i + 2, i + 1);
            } else dslot(n, i + 2, i + 1);
            if (i == 2 && n == 0) { st_store(mystat); u_store(bid * 8 + w, myfrag); }
            if (!A && i == 4 && n == 0) { if (w == 7) __builtin_amdgcn_raw_buffer_store_b64(tagv, wsr, l == 0 ? 0 : OOB, WS_E + bid * 8, 16); }
          } else if (i == 8) {
            if (n == 0) { if (ext0) dslot(0, 0, 9); }
            else if (n < 6) w2_dma(4, n - 1, slotL0);
            else if (n < (A ? 11 : 13)) w2_reg1(n - 6);
          } else {
            if (n < 5) w2_dma(5, n, slotL1);
            else if (n - 5 + (A ? 5 : 7) < NEARLY) w2_reg1(n - 5 + (A ? 5 : 7));
          }
        };
        constexpr int gap = (i == 1 || i >= 8) ? (A ? 4 : 3) : (A ? 6 : 5), per = (i == 1 || i >= 8) ? (A ? 6 : 7) : 4;
        step(Fb, Fa, i, 0, true, gap, [&](int n) { if (n < per) mem(n); });
        step(Fa, Fb, i, 1, true, gap, [&](int n) { if (n < per) mem(n + per); });
      });
      WAIT_LGKM0(); BAR();
      ST(1, 11);
#pragma unroll
      for (int b = 0; b < NB; b++) {
        const int sl = min(max(b == 5 ? t0 + (ffn ? 155 : -16) + c : t0 + 16 * (B0 + b) + c, 0), 2047);
        stv[b] = __builtin_amdgcn_raw_buffer_load_b64(wsr, sl * 8, WS_ST, 16);
      }
      SB();
      step(Fb, Fa, 0, 0, false, 4, [&](int n) { if (n < 5) w2_dma(6, n, slotL2); });
      __builtin_amdgcn_s_setprio(0);
      asm volatile("s_nop 7\n\ts_nop 1");
      SB();
    }
    ST(0, 3); ST(1, 12);
    relane();
    if constexpr (A) {
      const int cw1 = CS_OFF + s * 256 + g * 16, cw2 = V_OFF1 + s * 2048 + l * 16;
      LAS char* cw = smem + (c == 4 ? cw1 : cw2);
#pragma unroll
      for (int T = 0; T < 4; T++) *(LAS f32x4*)(cw + T * 64) = acc[5][T];
    }
    WAIT_LGKM0(); BAR();
    f32x4 cs[4];
#pragma unroll
    for (int T = 0; T < 4; T++) cs[T] = *(LAS f32x4*)(smem + CS_OFF + (s * 4 + T) * 64 + g * 16);
    ST(0, 4);
    int role = s32;
    asm volatile("" : "+s"(role));
    if (role < 18) {
      const int hso = WS_H + (20 + grp * 4 + s) * 4 * 2048 * 16;
      const float c0 = -2.f * 0.7978845608028654f * 1.4426950408889634f, c1 = c0 * 0.044715f;
#pragma unroll
      for (int b = 0; b < NB; b++) {
        asm volatile("" : "+v"(stv[b]));
        const float R = __uint_as_float(stv[b][1]), rs = __builtin_amdgcn_rsqf(R);
        const f32x2 nm = bc2(-__uint_as_float(stv[b][0])), B2 = bc2(c0 * rs), A2 = bc2(c1 * rs * rs * rs), R2 = bc2(R);
        u32x4 o;
#pragma unroll
        for (int h = 0; h < 4; h++) {
          const int t = h >> 1;
          const f32x2 gg = fma2(nm, h & 1 ? hi2(cs[t]) : lo2(cs[t]), h & 1 ? hi2(acc[b][t]) : lo2(acc[b][t]));
          const f32x2 uu = fma2(nm, h & 1 ? hi2(cs[2 + t]) : lo2(cs[2 + t]), h & 1 ? hi2(acc[b][2 + t]) : lo2(acc[b][2 + t]));
          const f32x2 ar = gg * fma2(gg * gg, A2, B2);
          const f32x2 e = {__builtin_amdgcn_exp2f(ar[0]), __builtin_amdgcn_exp2f(ar[1])};
          const f32x2 d = fma2(e, R2, R2);
          const f32x2 r = {__builtin_amdgcn_rcpf(d[0]), __builtin_amdgcn_rcpf(d[1])};
          o[h] = pk2(gg * uu * r);
          const int n = NEARLY + (A ? 3 * b + h : 2 * b + (h >> 1));
          if ((A ? h < 3 : (h & 1)) && n < 25) { SB(); w2_reg1(n); SB(); }
        }
        const int slot = b == 5 ? t0 + 155 + c : t0 + 16 * (B0 + b) + c;
        const bool ok = slot < tend && (b != 5 || c >= 5);
        st128(o, wsr, ok ? (g * 2048 + slot) * 16 : OOB, hso);
        SB();
      }
#pragma unroll
      for (int b = 0; b < NB; b++)
#pragma unroll
        for (int T = 0; T < 4; T++) asm volatile("" : "=v"(acc[b][T]));
      ST(0, 5);
      __builtin_amdgcn_s_waitcnt(0x0F70);
    }
    asm volatile("" : "+s"(role));
    if (role >= 18) {
      u32x4 Kf[6], Qf[5];
      float cq[5];
      const int vq1 = (0x3105 >> (4 * g)) & 15, vq2 = (0x5425 >> (4 * g)) & 15;
      const int vreg = s < 2 ? V_OFF0 : V_OFF1, voff = vreg + (s & 1) * V_HEAD, kx = vreg + 2 * V_HEAD + (s & 1) * 1024 + l * 16;
#pragma unroll
      for (int b = 0; b < NB; b++) {
        const int vb = b == 5 ? 0 : B0 + b + 1;
        asm volatile("" : "+v"(stv[b]));
        const float rs = __builtin_amdgcn_rsqf(__uint_as_float(stv[b][1]));
        const f32x2 nm = bc2(-__uint_as_float(stv[b][0])), r2 = bc2(rs), f2 = bc2(g == 0 ? 1.f : rs);
        u32x2 P[4];
#pragma unroll
        for (int T = 0; T < 4; T++) {
          if (b == 5 && T == 3) break;
          f32x2 ya = fma2(nm, lo2(cs[T]), lo2(acc[b][T])), yb = fma2(nm, hi2(cs[T]), hi2(acc[b][T]));
          if (T < 2) { ya *= r2; yb *= r2; }
          if (T == 2) { ya *= f2; yb *= f2; }
          P[T] = (u32x2){pk2(ya), pk2(yb)};
          if (T == 1 && b < 5) { SB(); w2_reg1(NEARLY + b); SB(); }
        }
        LAS char* vrow = smem + voff + (vb * 16 + c) * 48;
        *(LAS u32x2*)(vrow + vq1 * 8) = P[1];
        *(LAS u32x2*)(vrow + vq2 * 8) = P[2];
        Kf[b] = (u32x4){P[0][0], P[0][1], g == 0 ? P[1][0] : 0u, g == 0 ? P[1][1] : 0u};
        if (b < 5) {
          Qf[b] = (u32x4){g == 0 ? P[2][0] : P[3][0], g == 0 ? P[2][1] : P[3][1], P[3][0], P[3][1]};
          cq[b] = rs * (0.22360679774997896f * 1.4426950408889634f);
          asm volatile("" : "+v"(Qf[b]), "+v"(cq[b]));
        }
        asm volatile("" : "+v"(Kf[b]));
        SB();
      }
      if constexpr (A) *(LAS u32x4*)(smem + kx) = Kf[4];
      WAIT_LGKM0(); BAR();
      if constexpr (!A) Kf[5] = *(LAS u32x4*)(smem + kx);
      const bool usePrev = 4 * g + 3 >= c + 9;
      f32x4 Cm;
#pragma unroll
      for (int j = 0; j < 4; j++) {
        const int kk = 4 * g + j - (usePrev ? 16 : 0);
        Cm[j] = (unsigned)(c - kk) < 8u ? 0.f : -1e30f;
      }
      LAS char* vbase = smem + voff + (B0 * 16 + 4 * g + q) * 48;
      const int Q0 = 5 * s + g, Q1 = 5 * s + 4;
      LAS char* ob0 = smem + O_OFF0 + (Q0 >> 1) * 2560 + (Q0 >> 4) * (O_OFF1 - O_OFF0 - 8 * 2560) + (Q0 & 1) * 8 + (B0 * 16 + c) * 16;
      const int o1 = O_OFF0 + (Q1 >> 1) * 2560 + (Q1 >> 4) * (O_OFF1 - O_OFF0 - 8 * 2560) + (Q1 & 1) * 8 + (B0 * 16 + c) * 16, o1j = V_OFF1 + 8192 + tid * 8;
      const int o1s = g == 0 ? 256 : 0;
      LAS char* ob1 = smem + (g == 0 ? o1 : o1j);
      f32x4 Sp[5], Sc[5];
#pragma unroll
      for (int b = 0; b < 5; b++) { Sc[b] = mfma(Kf[b], Qf[b], Cm); if (b > 0) Sp[b] = mfma(Kf[b - 1], Qf[b], Cm); }
      Sp[0] = mfma(Kf[5], Qf[0], Cm);
      s16x4 Vt[6][2];
#pragma unroll
      for (int b = 0; b < 6; b++) { Vt[b][0] = lds_tr(vbase + b * 16 * 48 + p * 8); Vt[b][1] = lds_tr(vbase + b * 16 * 48 + 32); }
      SB();
      f32x4 sv[5];
      float mx[5];
#pragma unroll
      for (int b = 0; b < 5; b++) {
        if (((t0 + 16 * (B0 + b)) & 1023) == 0) Sp[b] = (f32x4){-1e30f, -1e30f, -1e30f, -1e30f};
#pragma unroll
        for (int j = 0; j < 4; j++) sv[b][j] = usePrev ? Sp[b][j] : Sc[b][j];
        mx[b] = fmx(fmx(fmx(sv[b][0], sv[b][1]), sv[b][2]), sv[b][3]);
      }
#pragma unroll
      for (int b = 0; b < 5; b++) { auto r = __builtin_amdgcn_permlane16_swap(__float_as_uint(mx[b]), __float_as_uint(mx[b]), false, false); mx[b] = fmx(__uint_as_float(r[0]), __uint_as_float(r[1])); }
#pragma unroll
      for (int b = 0; b < 5; b++) { auto r = __builtin_amdgcn_permlane32_swap(__float_as_uint(mx[b]), __float_as_uint(mx[b]), false, false); mx[b] = fmx(__uint_as_float(r[0]), __uint_as_float(r[1])); }
      u32x4 Pf[5];
#pragma unroll
      for (int b = 0; b < 5; b++) {
        const f32x2 cq2 = bc2(cq[b]), mc = bc2(-mx[b] * cq[b]);
        const f32x2 aa = fma2(lo2(sv[b]), cq2, mc), ab = fma2(hi2(sv[b]), cq2, mc);
        const unsigned p0 = pk2(__builtin_amdgcn_exp2f(aa[0]), __builtin_amdgcn_exp2f(aa[1])), p1 = pk2(__builtin_amdgcn_exp2f(ab[0]), __builtin_amdgcn_exp2f(ab[1]));
        Pf[b] = (u32x4){usePrev ? p0 : 0u, usePrev ? p1 : 0u, usePrev ? 0u : p0, usePrev ? 0u : p1};
        SB(); w2_reg1(NEARLY + 5 + b); SB();
      }
      WAIT_LGKM0(); BAR();
      const f32x4 z = {0.f, 0.f, 0.f, 0.f};
      const u32x4 ones = {0x3F803F80u, 0x3F803F80u, 0x3F803F80u, 0x3F803F80u};
      f32x4 O0[5], O1[5], L[5];
#pragma unroll
      for (int b = 0; b < 5; b++) {
        L[b] = mfma(ones, Pf[b], z);
        O0[b] = mfma(cat(Vt[b][0], Vt[b + 1][0]), __builtin_bit_cast(b16x8, Pf[b]), z);
        O1[b] = mfma(cat(Vt[b][1], Vt[b + 1][1]), __builtin_bit_cast(b16x8, Pf[b]), z);
        if constexpr (A) { SB(); w2_reg1(20 + b); SB(); }
      }
#pragma unroll
      for (int b = 0; b < 5; b++) {
        const f32x2 r = bc2(__builtin_amdgcn_rcpf(L[b][0]));
        *(LAS u32x2*)(ob0 + b * 256) = (u32x2){pk2(lo2(O0[b]) * r), pk2(hi2(O0[b]) * r)};
        *(LAS u32x2*)(ob1 + b * o1s) = (u32x2){pk2(lo2(O1[b]) * r), pk2(hi2(O1[b]) * r)};
      }
      WAIT_LGKM0(); BAR();
#pragma unroll
      for (int i = 0; i < 4; i++) {
        const int n = tid + 512 * i;
        if (i < 3 || (A && w == 0)) {
          const int kg = (n * 6554) >> 20, sl = n - kg * 160;
          const u32x4 v = *(LAS u32x4*)(smem + O_OFF0 + n * 16 + (kg >> 3) * (O_OFF1 - O_OFF0 - 8 * 2560));
          st128(v, wsr, t0 + sl < tend ? (kg * 2048 + t0 + sl) * 16 : OOB, WS_H + grp * 10 * 2048 * 16);
        }
      }
      ST(0, 5);
      __builtin_amdgcn_s_waitcnt(0x0F70);
    }
    wait_vm<0>(); WAIT_LGKM0(); BAR();
    ST(0, 6);
    if (!A && w == 7) __builtin_amdgcn_raw_buffer_store_b64(tagv, wsr, l == 0 ? 0 : OOB, (ffn ? WS_FF : WS_FA) + tile * 128 + grp * 8, 16);
  }
  relane(); geo();
  if constexpr (!A) {
    if (w == 7) {
      const int r0 = tt * 64, r1 = r0 + 63;
      const int fa = (3 * r0 + 2) >> 9, fb = (3 * r1 + 2) >> 9, aa = r0 / 160, ab = r1 / 160;
      const int lf = l < 12 ? fa : fb, la = l < 32 ? aa : ab, jf = l < 12 ? l : l - 12;
      const int of = WS_FF + lf * 128 + jf * 8, oa = WS_FA + la * 128 + (l & 7) * 8;
      const int off = l < 24 ? of : oa;
#pragma unroll 1
      for (unsigned n = 0; n < SPIN_CAP; n++) {
        u32x2 f;
        asm volatile("global_load_dwordx2 %0, %1, %2 sc1\n\ts_waitcnt vmcnt(0)" : "=&v"(f) : "v"(off), "s"(WS) : "memory");
        if (__builtin_amdgcn_ballot_w64(((f[0] ^ tagv[0]) | (f[1] ^ tagv[1])) != 0) == 0) break;
        __builtin_amdgcn_s_sleep(1);
      }
    }
  } else {
#pragma unroll
    for (int i = 0; i < 5; i++) w2_dma(7, i, slotL3);
  }
  ST(0, 7);
  BAR();
  ST(0, 8); ST(3, 1);
  f32x4 ac2[4][5];
#pragma unroll
  for (int b = 0; b < 4; b++)
#pragma unroll
    for (int T = 0; T < 5; T++) ac2[b][T] = (f32x4){0.f, 0.f, 0.f, 0.f};
  u32x4 hf[NS][4];
  const int hvo = (g * 2048 + tt * 64 + c) * 16;
  auto h_load1 = [&](int j, int b) { hf[j][b] = __builtin_amdgcn_raw_buffer_load_b128(wsr, hvo, WS_H + ks_of(j) * 4 * 2048 * 16 + ((b + s + 1) & 3) * 256, 16); };
  u32x2 xres[5];
  const int ooff = row_off(tt * 64 + s * 16 + c) + (ct * 80 + g * 4) * 2;
#define HD 2
#pragma unroll
  for (int j = 0; j < HD; j++)
#pragma unroll
    for (int b = 0; b < 4; b++) h_load1(j, b);
  SB();
  sfor<0, NS>([&](auto jc) {
    constexpr int j = decltype(jc)::value;
    ST(3, 2 + j);
    if (j == 0) __builtin_amdgcn_s_setprio(3);
    if (j == 2) __builtin_amdgcn_s_setprio(2);
    if (j == 4) __builtin_amdgcn_s_setprio(1);
    if (j == (A ? 6 : 7)) __builtin_amdgcn_s_setprio(0);
    constexpr bool reg = j < 4 || j == NS - 1;
    u32x4 wf[5];
    if constexpr (!reg) {
      const int sl = j == 4 ? slotL0 : j == 5 ? slotL1 : j == 6 ? slotL2 : slotL3;
      asm volatile("" ::: "memory");
#pragma unroll
      for (int T = 0; T < 5; T++) wf[T] = *(LAS u32x4*)(smem + sl + T * 1024 + l * 16);
    }
    if constexpr (A && j == NS - 1) { BAR(); }
#pragma unroll
    for (int m = 0; m < 20; m++) {
      const int b = m / 5, T = m % 5;
      if (T == 0 && j + HD < NS) h_load1(j + HD, b);
      ac2[b][T] = mfma(reg ? Wr[j < 4 ? j : 4][T] : wf[T], hf[j][b], ac2[b][T]);
      if (A && j == NS - 2 && (m & 3) == 1) xres[m >> 2] = *(const u32x2*)(X + ooff + (m >> 2) * 32);
      if (j == NS - 1 && m >= 5) {
        const int bb = (m - 5) / 5;
        *(LAS f32x4*)(smem + (bb == 0 ? slotL0 : bb == 1 ? slotL1 : slotL2) + T * 1024 + l * 16) = ac2[bb][T];
      }
      SB();
    }
  });
  ST(0, 9); ST(3, 11);
  auto gather = [&](int i) {
    const int v = (s + i) & 3;
    const int sv = i == 3 ? (A ? 4 + v : (v < 3 ? 16 + v : 26)) : i == 2 ? (A ? 8 + v : (v < 3 ? 19 + v : 27)) : (A ? v : 28 + v);
    LAS char* pp = smem + sv * 5120 + l * 16;
#pragma unroll
    for (int T = 0; T < 5; T++) ac2[3][T] += *(LAS f32x4*)(pp + T * 1024);
  };
  if constexpr (!A) {
    WAIT_LGKM0(); BAR();
#pragma unroll
    for (int i = 1; i < 4; i++) gather(i);
#pragma unroll
    for (int T = 0; T < 5; T++) *(LAS f32x4*)(smem + slotL3 + T * 1024 + l * 16) = ac2[3][T];
    WAIT_LGKM0(); BAR();
  } else {
    WAIT_LGKM0(); BAR();
    ST(0, 10); ST(3, 12);
#pragma unroll
    for (int i = 1; i < 4; i++) gather(i);
#pragma unroll
    for (int T = 0; T < 5; T++) {
      f32x4 a = ac2[3][T] + *(LAS f32x4*)(smem + (22 + s) * 5120 + T * 1024 + l * 16);
      a[0] += __uint_as_float(xres[T][0] << 16); a[1] += __uint_as_float(xres[T][0] & 0xffff0000u);
      a[2] += __uint_as_float(xres[T][1] << 16); a[3] += __uint_as_float(xres[T][1] & 0xffff0000u);
      __builtin_amdgcn_raw_buffer_store_b64((u32x2){pk2(lo2(a)), pk2(hi2(a))}, __builtin_amdgcn_make_buffer_rsrc(Out, 0, 2048 * 1280, 0x00020000), ooff + T * 32, 0, OUTAUX);
    }
  }
}

__global__ __launch_bounds__(512, 2) void k_fused(const char* __restrict__ X, const char* __restrict__ Wqkv, const char* __restrict__ Wo,
                                                  const char* __restrict__ Wg, const char* __restrict__ Wu, const char* __restrict__ Wd,
                                                  char* __restrict__ Out, char* __restrict__ WS, const Roles roles) {
  __shared__ __attribute__((aligned(16))) char smem_[LDS_BYTES];
  const char* code;
  asm volatile("s_getpc_b64 %0" : "=s"(code));
#define STARG
  const unsigned role_w = roles.r[blockIdx.x];
  asm volatile("" ::"s"(X), "s"(Wqkv), "s"(Wo), "s"(Wg), "s"(Wu), "s"(Wd), "s"(Out), "s"(WS), "s"(role_w));
  const int w = __builtin_amdgcn_readfirstlane(threadIdx.x >> 6);
  if (w < 4) body<true>(X, Wqkv, Wo, Wg, Wu, Wd, Out, WS, role_w, (LAS char*)smem_, w, code STARG);
  else body<false>(X, Wqkv, Wo, Wg, Wu, Wd, Out, WS, role_w, (LAS char*)smem_, w, code STARG);
}

extern "C" void kernel_launch(void* const* d_in, const int* in_sizes, int n_in, void* d_out, int out_size, void* d_ws, size_t ws_size,
                              hipStream_t stream) {
  Roles roles;
  for (int b = 0; b < 256; b++) {
    const int xj = b & 7, s32 = b >> 3, odd = xj & 1;
    int t0 = 0, n = 0, grp = 0, tile = 0;
    if (s32 < 18) {
      grp = 3 * (xj >> 1) + s32 % 3; tile = 6 * odd + s32 / 3;
      t0 = 512 * tile / 3; n = 512 * (tile + 1) / 3 - t0;
    } else if (s32 < 31) {
      const int idx = s32 - 18;
      grp = (xj & 6) + (idx >= 7 - odd); tile = odd ? (idx >= 6 ? idx : idx + 7) : (idx >= 7 ? idx - 7 : idx);
      t0 = 160 * tile; n = t0 + 160 > 2048 ? 2048 - t0 : 160;
    }
    roles.r[b] = t0 | n << 11 | grp << 19 | tile << 23;
  }
  k_fused<<<256, 512, 0, stream>>>((const char*)d_in[0], (const char*)d_in[1], (const char*)d_in[2], (const char*)d_in[4], (const char*)d_in[5],
                                   (const char*)d_in[6], (char*)d_out, (char*)d_ws, roles);
}
```

```cpp
#define KCHAIN
#include <hip/hip_runtime.h>
#include <stdint.h>
#include <type_traits>

#define LAS __attribute__((address_space(3)))
#define DI __device__ __forceinline__
typedef short s16x4 __attribute__((ext_vector_type(4)));
typedef __bf16 b16x2 __attribute__((ext_vector_type(2)));
typedef __bf16 b16x8 __attribute__((ext_vector_type(8)));
typedef float f32x2 __attribute__((ext_vector_type(2)));
typedef float f32x4 __attribute__((ext_vector_type(4)));
typedef unsigned u32x2 __attribute__((ext_vector_type(2)));
typedef unsigned u32x4 __attribute__((ext_vector_type(4)));
typedef unsigned long long u64;

constexpr int LDS_BYTES = 163840;
constexpr int XBUF = 20480, EB0 = 61440, EBUF = 1536, JUNK = 64512, WB0 = 65536, WBUF = 16384, WHALF = 49152;
constexpr int V_OFF0 = 61440, V_OFF1 = 112640, V_HEAD = 176 * 48;
constexpr int CS_OFF = V_OFF0 + 2 * V_HEAD + 2048;
constexpr int O_OFF0 = V_OFF0, O_OFF1 = V_OFF1;
constexpr int WS_E = 4096, WS_FF = 8192, WS_FA = 12288, WS_ST = 16384, WS_W2 = 65536, WS_H = 4194304, WS_BYTES = WS_H + 272 * 2048 * 16;
constexpr int OOB = 0x7FFFFFF0;
constexpr unsigned SPIN_CAP = 1u << 16;
#define CODE_LINES 12

template <int I, int N, class F> DI void sfor(F&& f) {
  if constexpr (I < N) { f(std::integral_constant<int, I>{}); sfor<I + 1, N>(f); }
}
DI unsigned pk2(float a, float b) { f32x2 v = {a, b}; return __builtin_bit_cast(unsigned, __builtin_convertvector(v, b16x2)); }
DI unsigned pk2(f32x2 v) { return __builtin_bit_cast(unsigned, __builtin_convertvector(v, b16x2)); }
DI f32x2 lo2(f32x4 v) { return __builtin_shufflevector(v, v, 0, 1); }
DI f32x2 hi2(f32x4 v) { return __builtin_shufflevector(v, v, 2, 3); }
DI f32x2 bc2(float v) { return (f32x2){v, v}; }
DI f32x2 fma2(f32x2 a, f32x2 b, f32x2 c) { return __builtin_elementwise_fma(a, b, c); }
DI f32x4 mfma(b16x8 a, b16x8 b, f32x4 c) { return __builtin_amdgcn_mfma_f32_16x16x32_bf16(a, b, c, 0, 0, 0); }
DI f32x4 mfma(u32x4 a, u32x4 b, f32x4 c) { return mfma(__builtin_bit_cast(b16x8, a), __builtin_bit_cast(b16x8, b), c); }
DI void mfma_acc(f32x4& c, b16x8 a, b16x8 b) { asm volatile("v_mfma_f32_16x16x32_bf16 %0, %1, %2, %0" : "+v"(c) : "v"(a), "v"(b)); }
DI b16x8 lds_b128(LAS char* p) { return *(LAS b16x8*)p; }
DI s16x4 lds_tr(LAS char* p) { return __builtin_amdgcn_ds_read_tr16_b64_v4i16((LAS s16x4*)p); }
DI b16x8 cat(s16x4 a, s16x4 b) { return __builtin_bit_cast(b16x8, __builtin_shufflevector(a, b, 0, 1, 2, 3, 4, 5, 6, 7)); }
DI b16x8 lds_tr2(LAS char* p, int d) { return cat(lds_tr(p), lds_tr(p + d)); }
DI void dmas(unsigned voff, const char* sb, unsigned dst) {
  asm volatile("s_mov_b32 m0, %2\n\ts_nop 0\n\tglobal_load_lds_dwordx4 %0, %1" ::"v"(voff), "s"(sb), "s"(dst) : "memory");
}
DI void dmas_sc1(unsigned voff, const char* sb, unsigned dst) {
  asm volatile("s_mov_b32 m0, %2\n\ts_nop 0\n\tglobal_load_lds_dwordx4 %0, %1 sc1" ::"v"(voff), "s"(sb), "s"(dst) : "memory");
}
DI void dmas_masked(unsigned voff, const char* sb, unsigned dst, u64 mask) {
  asm volatile("s_mov_b32 m0, %2\n\ts_mov_b64 exec, %3\n\tglobal_load_lds_dwordx4 %0, %1\n\ts_mov_b64 exec, -1" ::"v"(voff), "s"(sb), "s"(dst), "s"(mask) : "memory");
}
DI void touch(const char* a) { asm volatile("s_mov_b32 m0, %1\n\ts_nop 0\n\tglobal_load_lds_dword %0, off" ::"v"(a), "n"(JUNK) : "memory"); }
template <class R> DI void st128(u32x4 v, R rsrc, int voff, int soff) {
  __builtin_amdgcn_raw_buffer_store_b128(v, rsrc, voff, soff, 16);
  asm volatile("s_nop 1" ::"v"(v) : "memory");
}
template <int N> DI void wait_vm() { asm volatile("s_waitcnt vmcnt(%0)" ::"n"(N) : "memory"); }
#define WAIT_LGKM0() do { asm volatile("" ::: "memory"); __builtin_amdgcn_s_waitcnt(0xC07F); asm volatile("" ::: "memory"); } while (0)
#define BAR() do { asm volatile("" ::: "memory"); __builtin_amdgcn_s_barrier(); asm volatile("" ::: "memory"); } while (0)
#define SB() __builtin_amdgcn_sched_barrier(0)
DI float fmx(float a, float b) { return __builtin_elementwise_maximum(a, b); }
DI int row_off(int slot) { return slot * 2560 - (slot >> 10) * (2047 * 1280); }
DI float xrow_sum(float v) {
  unsigned u = __float_as_uint(v);
  auto r = __builtin_amdgcn_permlane16_swap(u, u, false, false);
  v = __uint_as_float(r[0]) + __uint_as_float(r[1]);
  u = __float_as_uint(v);
  auto r2 = __builtin_amdgcn_permlane32_swap(u, u, false, false);
  return __uint_as_float(r2[0]) + __uint_as_float(r2[1]);
}
template <int C> DI float dpp_add(float v) { return v + __uint_as_float(__builtin_amdgcn_update_dpp(0, __float_as_uint(v), C, 0xf, 0xf, true)); }
DI float wave_sum(float v) { return xrow_sum(dpp_add<0x121>(dpp_add<0x122>(dpp_add<0x124>(dpp_add<0x128>(v))))); }

#define ST(set, k)

struct Frag { b16x8 x[6]; s16x4 w[4][2]; };
struct Roles { unsigned r[256]; };

template <bool A>
DI void body(const char* __restrict__ X, const char* __restrict__ Wqkv, const char* __restrict__ Wo, const char* __restrict__ Wg,
             const char* __restrict__ Wu, const char* __restrict__ Wd, char* __restrict__ Out, char* __restrict__ WS, const unsigned role_w,
             LAS char* smem, const int w, const char* code
) {
  constexpr int NB = A ? 6 : 5, B0 = A ? 0 : 5, NS = A ? 9 : 8;
  int tid = threadIdx.x, l = tid & 63;
  const int s = w & 3;
  int g = l >> 4, c = l & 15, q = c >> 2, p = l & 3;
  auto relane = [&]() { tid = threadIdx.x; asm volatile("" : "+v"(tid)); l = tid & 63; g = l >> 4; c = l & 15; q = c >> 2; p = l & 3; };
  const int bid = blockIdx.x, xj = bid & 7, s32 = bid >> 3;
  const bool idle = s32 == 31, ffn = s32 < 18;
  const auto wsr = __builtin_amdgcn_make_buffer_rsrc(WS, 0, WS_BYTES, 0x00020000);
  const int t0 = role_w & 2047, tend = t0 + ((role_w >> 11) & 255), grp = (role_w >> 19) & 15, tile = role_w >> 23;

  auto tx_addr = [&]() { return X + row_off(min((xj & 1) * 944 + s32 * 35 + min(l, 34), 2047)); };
  auto tw_addr = [&](int& stride64) {
    const int m = s32 * 42 + min(l, 41), ma = m - 768, ra = (ma * 7282) >> 16, r9 = ma - 9 * ra, mt = (r9 * 11) >> 5;
    const char* f = ((m & 2) ? Wu : Wg) + ((m & 255) >> 2) * 3072 + (3 * (xj >> 1) + (m >> 8)) * 256 + (m & 1) * 128;
    const char* a = Wqkv + ra * 3840 + mt * 1280 + ((5 * (xj >> 1)) >> 1) * 128 + (r9 - 3 * mt) * 128;
    stride64 = m < 768 ? 64 * 3072 : 64 * 3840;
    return m < 768 ? f : a;
  };
#define STRICT1 0
#define TMODE 2
  auto touch1 = [&](int c0) {
    const char* ta;
    if (w & 1) { int st; ta = tw_addr(st); ta += ((w >> 1) + c0) * st; }
    else ta = tx_addr() + ((w >> 1) + c0) * 128;
    touch(ta);
  };
  if (CODE_LINES && A) { if (w == 0) touch(code + (s32 * CODE_LINES + min(l, CODE_LINES - 1)) * 128); }
  if (TMODE == 1) touch1(0);
  auto row_issue = [&](u32x4& sa, unsigned& sb1) {
    asm volatile("global_load_dwordx4 %0, %2, %4\n\tglobal_load_dword %1, %3, %4 offset:1024" : "=&v"(sa), "=&v"(sb1) : "v"(l * 16), "v"(l * 4), "s"(X + row_off(8 * bid + w)) : "memory");
    SB();
  };
  auto row_stats = [&](u32x4 sa, unsigned sb1) {
    float ss = 0.f, qq = 0.f;
#pragma unroll
    for (int i = 0; i < 5; i++) {
      const unsigned d = i < 4 ? sa[i] : sb1;
      const float lo = __uint_as_float(d << 16), hi = __uint_as_float(d & 0xffff0000u);
      ss += lo + hi; qq = fmaf(lo, lo, fmaf(hi, hi, qq));
    }
    ss = wave_sum(ss); qq = wave_sum(qq);
    const float m = ss * (1.f / 640.f);
    const float R = fmaxf(qq * (1.f / 640.f) - m * m, 0.f) + 1e-5f;
    return (u32x2){__float_as_uint(m), __float_as_uint(R)};
  };
  auto st_store = [&](u32x2 v) { __builtin_amdgcn_raw_buffer_store_b64(v, wsr, l == 0 ? 0 : OOB, WS_ST + (8 * bid + w) * 8, 16); };
  u32x2 tagv;
  auto get_tag = [&]() {
    const char* p0 = WS;
    asm volatile("" : "+s"(p0));
    const u64 tag = *(const __attribute__((address_space(4))) u64*)p0 + 0x9E3779B97F4A7C15ull;
    tagv = (u32x2){(unsigned)tag, (unsigned)(tag >> 32)};
  };
  auto u_dma = [&](int f, unsigned dst) {
    const int rho = l >> 1, qq = rho & 3, gg = ((rho >> 2) & 1) | ((rho >> 4) << 1), t2 = (rho >> 3) & 1;
    const int ks = f / 40, c16 = f - ks * 40;
    const char* base = (ks < 20 ? Wo + ks * 40960 : Wd + (ks - 20) * 40960) + c16 * 32;
    dmas(((ks < 20 ? 8 * gg + 4 * t2 : 16 * t2 + 4 * gg) + qq) * 1280 + (l & 1) * 16, base, dst);
  };
  auto u_read = [&](unsigned src) { return __builtin_bit_cast(u32x4, lds_tr2(smem + src + (q + 4 * (g & 1) + 16 * (g >> 1)) * 32 + p * 8, 256)); };
  auto u_store = [&](int f, u32x4 v) {
    const int ks = f / 40, c16 = f - ks * 40, c5 = c16 / 5;
    st128(v, wsr, l * 16, WS_W2 + ((c5 * 68 + ks) * 5 + (c16 - c5 * 5)) * 1024);
  };
  u32x4 e0, e1;
  auto e_issue = [&]() {
    asm volatile("global_load_dwordx4 %0, %2, %3 sc1\n\tglobal_load_dwordx4 %1, %2, %3 offset:16 sc1" : "=&v"(e0), "=&v"(e1) : "v"(l * 32), "s"(WS + WS_E) : "memory");
  };
  auto e_spin = [&]() {
#pragma unroll 1
    for (unsigned n = 0; n < SPIN_CAP; n++) {
      const unsigned d = ((e0[0] ^ tagv[0]) | (e0[1] ^ tagv[1]) | (e0[2] ^ tagv[0]) | (e0[3] ^ tagv[1])) | ((e1[0] ^ tagv[0]) | (e1[1] ^ tagv[1]) | (e1[2] ^ tagv[0]) | (e1[3] ^ tagv[1]));
      if (__builtin_amdgcn_ballot_w64(d != 0) == 0) break;
      __builtin_amdgcn_s_sleep(2);
      e_issue();
      asm volatile("s_waitcnt vmcnt(0)" : "+v"(e0), "+v"(e1)::"memory");
    }
  };
  int tt, ct, ks0, slotL0, slotL1, slotL2, slotL3;
  auto geo = [&]() {
    int s_ = s, b_ = bid;
    asm volatile("" : "+s"(s_), "+s"(b_));
    tt = (b_ & 7) * 4 + (b_ >> 6); ct = (b_ >> 3) & 7;
    ks0 = 17 * s_ + (A ? 0 : 9);
    slotL0 = (A ? 4 + s_ : (s_ < 3 ? 16 + s_ : 26)) * 5120; slotL1 = (A ? 8 + s_ : (s_ < 3 ? 19 + s_ : 27)) * 5120;
    slotL2 = (A ? s_ : 28 + s_) * 5120; slotL3 = (A ? 12 + s_ : 22 + s_) * 5120;
  };
#define ROT 0
  auto ks_of = [&](int j) {
    if (ROT == 0) return ks0 + j;
    const int k = j + 2 * (tt & 3);
    return ks0 + (k >= NS ? k - NS : k);
  };
  auto w2_dma = [&](int j, int i, int slot) { dmas_sc1(l * 16, WS + WS_W2 + ((ct * 68 + ks_of(j)) * 5 + i) * 1024, slot + i * 1024); };
  u32x4 Wr[5][5];
  auto w2_reg1 = [&](int n) { Wr[n / 5][n % 5] = __builtin_amdgcn_raw_buffer_load_b128(wsr, l * 16 + (n % 5) * 1024, WS_W2 + (ct * 68 + ks_of(n < 20 ? n / 5 : NS - 1)) * 5120, 16); };

  if (idle) {
    const int f1 = 2048 + xj * 84 + w;
    relane();
    if (TMODE == 2) touch1(1);
    u32x4 sa; unsigned sb1;
    row_issue(sa, sb1);
    get_tag(); geo();
    u_dma(bid * 8 + w, (65536 + w * 12288));
#pragma unroll 1
    for (int i = 0; i < (A ? 11 : 10); i++) u_dma(f1 + 8 * i, (65536 + w * 12288) + (i + 1) * 1024);
    asm volatile("s_waitcnt vmcnt(0)" : "+v"(sa), "+v"(sb1)::"memory");
    st_store(row_stats(sa, sb1));
    u_store(bid * 8 + w, u_read((65536 + w * 12288)));
#pragma unroll 1
    for (int i = 0; i < (A ? 11 : 10); i++) u_store(f1 + 8 * i, u_read((65536 + w * 12288) + (i + 1) * 1024));
    wait_vm<0>(); WAIT_LGKM0(); BAR();
    if (!A && w == 7) {
      __builtin_amdgcn_raw_buffer_store_b64(tagv, wsr, l == 0 ? 0 : OOB, WS_E + bid * 8, 16);
      e_issue();
      asm volatile("s_waitcnt vmcnt(0)" : "+v"(e0), "+v"(e1)::"memory");
      e_spin();
      if (xj == 0) __builtin_amdgcn_raw_buffer_store_b64(tagv, wsr, l == 0 ? 0 : OOB, 0, 16);
    }
    BAR();
#pragma unroll 1
    for (int i = 0; i < 5; i++) { w2_dma(4, i, slotL0); w2_dma(5, i, slotL1); w2_dma(6, i, slotL2); }
#pragma unroll
    for (int n = 0; n < 25; n++) w2_reg1(n);
  } else {
    u32x4 sa; unsigned sb1;
    row_issue(sa, sb1);
    unsigned vx[3], vw[2], dstx[3];
    const bool ext0 = !A && w >= 6;
    const int wstride = ffn ? 3072 : 3840;
    const u64 mask0 = (!A && w == 6) ? 0xFFFFFF0000000000ull : ~0ull;
    const unsigned vx0 = (l >> 3) * 2560 + (((l & 7) ^ g) << 4);
#pragma unroll
    for (int i = 0; i < 3; i++) {
      const int w3 = 3 * w + i, w14 = 14 + w, eadj = ffn ? -5 : -176;
      const int pc = A ? w3 : i == 0 ? (ext0 ? w14 : w3) : min(w3, 17 + i);
      int s0 = t0 + 8 * pc + (i == 0 && ext0 ? eadj : 0);
      if (!A) asm("s_max_i32 %0, %0, 0\n\ts_min_i32 %0, %0, 0x7f8" : "+s"(s0)::"scc");
      vx[i] = (vx0 ^ ((pc & 1) << 6)) + row_off(s0);
      dstx[i] = pc * 1024 + (i == 0 && ext0 ? EB0 - 512 - 20 * 1024 : 0);
      if (i == 0 && !A) dmas_masked(vx[0], X, dstx[0], mask0); else dmas(vx[i], X, dstx[i]);
      SB();
    }
#pragma unroll
    for (int i = 0; i < 2; i++) {
      const int lc = c ^ (g << 1) ^ (i << 3), la = lc + (A ? 0 : 16);
      const int m1 = la >= 10, m2 = la >= 21;
      const int ca = (m1 + m2) * 1280 + min(la - 10 * m1 - 11 * m2, 9) * 16, cf = lc * 16;
      vw[i] = g * wstride + (ffn ? cf : ca);
    }
    const char* wb = (ffn ? (A ? Wg : Wu) : Wqkv) + (unsigned)(grp * (ffn ? 256 : 160) + 16 * s * wstride);
    const unsigned dstw = WB0 + (A ? 0 : WHALF) + s * 4096;
    auto dslot = [&](int i, int ch, int che) {
      if (i == 0 && !A) dmas_masked(vx[0], X + (ext0 ? che : ch) * 128, dstx[0] + (ext0 ? (che & 1) * EBUF : (ch % 3) * XBUF), mask0);
      else if (i < 3) dmas(vx[i], X + ch * 128, dstx[i] + (ch % 3) * XBUF);
      else dmas(vw[(i - 3) >> 1], wb + (ch * 64 + (i - 3) * 4) * wstride, dstw + (i - 3) * 1024 + (ch % 3) * WBUF);
    };
    ST(2, 1);
#pragma unroll
    for (int i = 3; i < 7; i++) dslot(i, 0, 0);
    ST(2, 2);
    const unsigned ust = dstw + 3 * 1024 + 2 * WBUF;
    u_dma(bid * 8 + w, ust);
    if (TMODE == 2) touch1(1);
    get_tag(); geo();
    *(LAS unsigned*)(smem + EB0 + (l >> 5) * EBUF + (l & 31) * 4) = 0x3F803F80u;
    int xr[2], er[2], wr[4];
    {
      const int re = max(c, 4);
      xr[0] = c * 128 + ((g ^ (c >> 1)) << 4) + B0 * 2048; xr[1] = xr[0] ^ 64;
      er[0] = EB0 + (re - 4) * 128 + ((g ^ (re >> 1)) << 4); er[1] = er[0] ^ 64;
      const int f = q | ((g & 1) << 2);
#pragma unroll
      for (int T = 0; T < 4; T++) {
        const unsigned map = T == 0 ? 0x17161514u : T == 1 ? 0x2D2B2A18u : T == 2 ? 0x2E2E2C00u : 0x03020104u;
        const int ua = 5 * s + ((map >> (8 * p)) & 255), uf = 32 * (T >> 1) + 8 * s + 4 * (T & 1) + p;
        const int u = ffn ? uf : ua;
        wr[T] = WB0 + (u >> 5) * WHALF + (8 * g + q) * 256 + ((((u >> 1) & 15) ^ (f << 1)) << 4) + (u & 1) * 8;
      }
    }
    const char* tpx = X; const char* tpw = X; int tst = 0;
    if constexpr (!A) { tpx = tx_addr(); tpw = tw_addr(tst); }
    auto dslot7 = [&](int n, int ch, int che, int tch) {
      const int tc = tch + (TMODE == 2);
      if (TMODE && !A && tc < 10 && w == 7) { if (n == 1) touch(tpx + tc * 128); else touch(tpw + tc * tst); }
      else dslot(n, ch, che);
    };
    ST(2, 3);
    asm volatile("s_waitcnt vmcnt(%2)" : "+v"(sa), "+v"(sb1) : "n"(8 + (TMODE == 2) - STRICT1) : "memory");
    ST(2, 4);
    u32x2 mystat = row_stats(sa, sb1);
    asm volatile("" : "+v"(mystat));
    u32x4 myfrag;
    ST(2, 5);
#pragma unroll
    for (int i = 0; i < 3; i++) dslot(i, 1, 1);
    f32x4 acc[NB][4];
#pragma unroll
    for (int b = 0; b < NB; b++)
#pragma unroll
      for (int T = 0; T < 4; T++) acc[b][T] = (f32x4){0.f, 0.f, 0.f, 0.f};
#pragma unroll
    for (int b = 0; b < NB; b++) asm volatile("" : "+v"(acc[b][0]), "+v"(acc[b][1]), "+v"(acc[b][2]), "+v"(acc[b][3]));
    u32x2 stv[NB];
    constexpr int NEARLY = A ? 10 : 15;

    {
      constexpr int NM = 4 * NB, NR = 8 + NB;
      Frag Fa, Fb;
      auto rd = [&](Frag& F, int ch, int kk, int n) {
        if (n < 8) F.w[n >> 1][n & 1] = lds_tr(smem + wr[n >> 1] + (ch % 3) * WBUF + (kk * 32 + (n & 1) * 4) * 256);
        else if (n < 13) F.x[n - 8] = lds_b128(smem + xr[kk] + (ch % 3) * XBUF + (n - 8) * 2048);
        else F.x[5] = lds_b128(smem + er[kk] + (ch & 1) * EBUF);
      };
      auto step = [&](Frag& C, Frag& N, int ch, int kk, bool doload, int gap, auto&& mem) {
#pragma unroll
        for (int m = 0; m < NM; m++) {
          if (m == 0) { if (kk == 0) __builtin_amdgcn_s_setprio(3); else __builtin_amdgcn_s_setprio(1); }
          if (m == NM / 2) { if (kk == 0) __builtin_amdgcn_s_setprio(2); else __builtin_amdgcn_s_setprio(0); }
          mfma_acc(acc[m >> 2][m & 3], cat(C.w[m & 3][0], C.w[m & 3][1]), C.x[m >> 2]);
          if (doload && m < NR) rd(N, ch, kk, m);
          if (m % gap == gap - 2) mem(m / gap);
          SB();
        }
      };
      ST(2, 6);
      wait_vm<4 + (TMODE == 2)>(); ST(2, 7);
      WAIT_LGKM0(); BAR();
      ST(0, 1); ST(1, 1); ST(2, 8);
#pragma unroll
      for (int n = 0; n < NR; n++) rd(Fa, 0, 0, n);
      SB();
      ST(2, 9);
      step(Fa, Fb, 0, 1, true, A ? 3 : 2, [&](int n) {
        if (n < 4) dslot(n + 3, 1, 1);
        else if (n == 4) { if (!ext0) dslot(0, 2, 0); else dslot(1, 2, 0); }
        else if (n < 7) dslot7(n - 4, 2, 0, 4);
      });
      sfor<1, 10>([&](auto ic) {
        constexpr int i = decltype(ic)::value;
        if (i == 1) ST(2, 10);
        if constexpr (i == 5 && !A) { if (w == 7) wait_vm<7>(); else wait_vm<6>(); }
        else wait_vm<i == 1 ? 3 : i == 3 ? 8 : i == 9 ? (A ? 10 : 12) : 6>();
        if (i == 1) ST(2, 11);
        if constexpr (i == 8 && !A) {
          if (w == 7) { asm volatile("s_waitcnt vmcnt(4)" : "+v"(e0), "+v"(e1)::"memory"); e_spin(); }
        }
        WAIT_LGKM0(); BAR();
        ST(1, 1 + i);
        if (i == 5) ST(0, 2);
        if constexpr (i == 1) { myfrag = u_read(ust); SB(); }
        auto mem = [&](int n) {
          if (i == 1) {
            if (n == 0) dslot(0, 3, 2);
            else if (n < 5) { if (n == 4) asm volatile("" : "+v"(myfrag)); dslot(n + 2, 2, 0); }
            else if (n < 7) dslot7(n - 4, 3, 2, 5);
            else if (n < 11) dslot(n - 4, 3, 2);
          } else if (i <= 7) {
            if (n >= 7) return;
            if (n == 1 || n == 2) {
              if (i <= 5) dslot7(n, i + 2, i + 1, i + 4);
              else if (!A && i == 7) { if (w == 7) { if (n == 1) e_issue(); } else dslot(n, i + 2, i + 1); }
              else dslot(n, i + 2, i + 1);
            } else dslot(n, i + 2, i + 1);
            if (i == 2 && n == 0) { st_store(mystat); u_store(bid * 8 + w, myfrag); }
            if (!A && i == 4 && n == 0) { if (w == 7) __builtin_amdgcn_raw_buffer_store_b64(tagv, wsr, l == 0 ? 0 : OOB, WS_E + bid * 8, 16); }
          } else if (i == 8) {
            if (n == 0) { if (ext0) dslot(0, 0, 9); }
            else if (n < 6) w2_dma(4, n - 1, slotL0);
            else if (n < (A ? 11 : 13)) w2_reg1(n - 6);
          } else {
            if (n < 5) w2_dma(5, n, slotL1);
            else if (n - 5 + (A ? 5 : 7) < NEARLY) w2_reg1(n - 5 + (A ? 5 : 7));
          }
        };
        constexpr int gap = (i == 1 || i >= 8) ? (A ? 4 : 3) : (A ? 6 : 5), per = (i == 1 || i >= 8) ? (A ? 6 : 7) : 4;
        step(Fb, Fa, i, 0, true, gap, [&](int n) { if (n < per) mem(n); });
        step(Fa, Fb, i, 1, true, gap, [&](int n) { if (n < per) mem(n + per); });
      });
      WAIT_LGKM0(); BAR();
      ST(1, 11);
#pragma unroll
      for (int b = 0; b < NB; b++) {
        const int sl = min(max(b == 5 ? t0 + (ffn ? 155 : -16) + c : t0 + 16 * (B0 + b) + c, 0), 2047);
        stv[b] = __builtin_amdgcn_raw_buffer_load_b64(wsr, sl * 8, WS_ST, 16);
      }
      SB();
      step(Fb, Fa, 0, 0, false, 4, [&](int n) { if (n < 5) w2_dma(6, n, slotL2); });
      __builtin_amdgcn_s_setprio(0);
      asm volatile("s_nop 7\n\ts_nop 1");
      SB();
    }
    ST(0, 3); ST(1, 12);
    relane();
    if constexpr (A) {
      const int cw1 = CS_OFF + s * 256 + g * 16, cw2 = V_OFF1 + s * 2048 + l * 16;
      LAS char* cw = smem + (c == 4 ? cw1 : cw2);
#pragma unroll
      for (int T = 0; T < 4; T++) *(LAS f32x4*)(cw + T * 64) = acc[5][T];
    }
    WAIT_LGKM0(); BAR();
    f32x4 cs[4];
#pragma unroll
    for (int T = 0; T < 4; T++) cs[T] = *(LAS f32x4*)(smem + CS_OFF + (s * 4 + T) * 64 + g * 16);
    ST(0, 4);
    int role = s32;
    asm volatile("" : "+s"(role));
    if (role < 18) {
      const int hso = WS_H + (20 + grp * 4 + s) * 4 * 2048 * 16;
      const float c0 = -2.f * 0.7978845608028654f * 1.4426950408889634f, c1 = c0 * 0.044715f;
#pragma unroll
      for (int b = 0; b < NB; b++) {
        asm volatile("" : "+v"(stv[b]));
        const float R = __uint_as_float(stv[b][1]), rs = __builtin_amdgcn_rsqf(R);
        const f32x2 nm = bc2(-__uint_as_float(stv[b][0])), B2 = bc2(c0 * rs), A2 = bc2(c1 * rs * rs * rs), R2 = bc2(R);
        u32x4 o;
#pragma unroll
        for (int h = 0; h < 4; h++) {
          const int t = h >> 1;
          const f32x2 gg = fma2(nm, h & 1 ? hi2(cs[t]) : lo2(cs[t]), h & 1 ? hi2(acc[b][t]) : lo2(acc[b][t]));
          const f32x2 uu = fma2(nm, h & 1 ? hi2(cs[2 + t]) : lo2(cs[2 + t]), h & 1 ? hi2(acc[b][2 + t]) : lo2(acc[b][2 + t]));
          const f32x2 ar = gg * fma2(gg * gg, A2, B2);
          const f32x2 e = {__builtin_amdgcn_exp2f(ar[0]), __builtin_amdgcn_exp2f(ar[1])};
          const f32x2 d = fma2(e, R2, R2);
          const f32x2 r = {__builtin_amdgcn_rcpf(d[0]), __builtin_amdgcn_rcpf(d[1])};
          o[h] = pk2(gg * uu * r);
          const int n = NEARLY + (A ? 3 * b + h : 2 * b + (h >> 1));
          if ((A ? h < 3 : (h & 1)) && n < 25) { SB(); w2_reg1(n); SB(); }
        }
        const int slot = b == 5 ? t0 + 155 + c : t0 + 16 * (B0 + b) + c;
        const bool ok = slot < tend && (b != 5 || c >= 5);
        st128(o, wsr, ok ? (g * 2048 + slot) * 16 : OOB, hso);
        SB();
      }
#pragma unroll
      for (int b = 0; b < NB; b++)
#pragma unroll
        for (int T = 0; T < 4; T++) asm volatile("" : "=v"(acc[b][T]));
      ST(0, 5);
      __builtin_amdgcn_s_waitcnt(0x0F70);
    }
    asm volatile("" : "+s"(role));
    if (role >= 18) {
      u32x4 Kf[6], Qf[5];
      float cq[5];
      const int vq1 = (0x3105 >> (4 * g)) & 15, vq2 = (0x5425 >> (4 * g)) & 15;
      const int vreg = s < 2 ? V_OFF0 : V_OFF1, voff = vreg + (s & 1) * V_HEAD, kx = vreg + 2 * V_HEAD + (s & 1) * 1024 + l * 16;
#pragma unroll
      for (int b = 0; b < NB; b++) {
        const int vb = b == 5 ? 0 : B0 + b + 1;
        asm volatile("" : "+v"(stv[b]));
        const float rs = __builtin_amdgcn_rsqf(__uint_as_float(stv[b][1]));
        const f32x2 nm = bc2(-__uint_as_float(stv[b][0])), r2 = bc2(rs), f2 = bc2(g == 0 ? 1.f : rs);
        u32x2 P[4];
#pragma unroll
        for (int T = 0; T < 4; T++) {
          if (b == 5 && T == 3) break;
          f32x2 ya = fma2(nm, lo2(cs[T]), lo2(acc[b][T])), yb = fma2(nm, hi2(cs[T]), hi2(acc[b][T]));
          if (T < 2) { ya *= r2; yb *= r2; }
          if (T == 2) { ya *= f2; yb *= f2; }
          P[T] = (u32x2){pk2(ya), pk2(yb)};
          if (T == 1 && b < 5) { SB(); w2_reg1(NEARLY + b); SB(); }
        }
        LAS char* vrow = smem + voff + (vb * 16 + c) * 48;
        *(LAS u32x2*)(vrow + vq1 * 8) = P[1];
        *(LAS u32x2*)(vrow + vq2 * 8) = P[2];
        Kf[b] = (u32x4){P[0][0], P[0][1], g == 0 ? P[1][0] : 0u, g == 0 ? P[1][1] : 0u};
        if (b < 5) {
          Qf[b] = (u32x4){g == 0 ? P[2][0] : P[3][0], g == 0 ? P[2][1] : P[3][1], P[3][0], P[3][1]};
          cq[b] = rs * (0.22360679774997896f * 1.4426950408889634f);
          asm volatile("" : "+v"(Qf[b]), "+v"(cq[b]));
        }
        asm volatile("" : "+v"(Kf[b]));
        SB();
      }
      if constexpr (A) *(LAS u32x4*)(smem + kx) = Kf[4];
      WAIT_LGKM0(); BAR();
      if constexpr (!A) Kf[5] = *(LAS u32x4*)(smem + kx);
      const bool usePrev = 4 * g + 3 >= c + 9;
      f32x4 Cm;
#pragma unroll
      for (int j = 0; j < 4; j++) {
        const int kk = 4 * g + j - (usePrev ? 16 : 0);
        Cm[j] = (unsigned)(c - kk) < 8u ? 0.f : -1e30f;
      }
      LAS char* vbase = smem + voff + (B0 * 16 + 4 * g + q) * 48;
      const int Q0 = 5 * s + g, Q1 = 5 * s + 4;
      LAS char* ob0 = smem + O_OFF0 + (Q0 >> 1) * 2560 + (Q0 >> 4) * (O_OFF1 - O_OFF0 - 8 * 2560) + (Q0 & 1) * 8 + (B0 * 16 + c) * 16;
      const int o1 = O_OFF0 + (Q1 >> 1) * 2560 + (Q1 >> 4) * (O_OFF1 - O_OFF0 - 8 * 2560) + (Q1 & 1) * 8 + (B0 * 16 + c) * 16, o1j = V_OFF1 + 8192 + tid * 8;
      const int o1s = g == 0 ? 256 : 0;
      LAS char* ob1 = smem + (g == 0 ? o1 : o1j);
      f32x4 Sp[5], Sc[5];
#pragma unroll
      for (int b = 0; b < 5; b++) { Sc[b] = mfma(Kf[b], Qf[b], Cm); if (b > 0) Sp[b] = mfma(Kf[b - 1], Qf[b], Cm); }
      Sp[0] = mfma(Kf[5], Qf[0], Cm);
      s16x4 Vt[6][2];
#pragma unroll
      for (int b = 0; b < 6; b++) { Vt[b][0] = lds_tr(vbase + b * 16 * 48 + p * 8); Vt[b][1] = lds_tr(vbase + b * 16 * 48 + 32); }
      SB();
      f32x4 sv[5];
      float mx[5];
#pragma unroll
      for (int b = 0; b < 5; b++) {
        if (((t0 + 16 * (B0 + b)) & 1023) == 0) Sp[b] = (f32x4){-1e30f, -1e30f, -1e30f, -1e30f};
#pragma unroll
        for (int j = 0; j < 4; j++) sv[b][j] = usePrev ? Sp[b][j] : Sc[b][j];
        mx[b] = fmx(fmx(fmx(sv[b][0], sv[b][1]), sv[b][2]), sv[b][3]);
      }
#pragma unroll
      for (int b = 0; b < 5; b++) { auto r = __builtin_amdgcn_permlane16_swap(__float_as_uint(mx[b]), __float_as_uint(mx[b]), false, false); mx[b] = fmx(__uint_as_float(r[0]), __uint_as_float(r[1])); }
#pragma unroll
      for (int b = 0; b < 5; b++) { auto r = __builtin_amdgcn_permlane32_swap(__float_as_uint(mx[b]), __float_as_uint(mx[b]), false, false); mx[b] = fmx(__uint_as_float(r[0]), __uint_as_float(r[1])); }
      u32x4 Pf[5];
#pragma unroll
      for (int b = 0; b < 5; b++) {
        const f32x2 cq2 = bc2(cq[b]), mc = bc2(-mx[b] * cq[b]);
        const f32x2 aa = fma2(lo2(sv[b]), cq2, mc), ab = fma2(hi2(sv[b]), cq2, mc);
        const unsigned p0 = pk2(__builtin_amdgcn_exp2f(aa[0]), __builtin_amdgcn_exp2f(aa[1])), p1 = pk2(__builtin_amdgcn_exp2f(ab[0]), __builtin_amdgcn_exp2f(ab[1]));
        Pf[b] = (u32x4){usePrev ? p0 : 0u, usePrev ? p1 : 0u, usePrev ? 0u : p0, usePrev ? 0u : p1};
        SB(); w2_reg1(NEARLY + 5 + b); SB();
      }
      WAIT_LGKM0(); BAR();
      const f32x4 z = {0.f, 0.f, 0.f, 0.f};
      const u32x4 ones = {0x3F803F80u, 0x3F803F80u, 0x3F803F80u, 0x3F803F80u};
      f32x4 O0[5], O1[5], L[5];
#pragma unroll
      for (int b = 0; b < 5; b++) {
        L[b] = mfma(ones, Pf[b], z);
        O0[b] = mfma(cat(Vt[b][0], Vt[b + 1][0]), __builtin_bit_cast(b16x8, Pf[b]), z);
        O1[b] = mfma(cat(Vt[b][1], Vt[b + 1][1]), __builtin_bit_cast(b16x8, Pf[b]), z);
        if constexpr (A) { SB(); w2_reg1(20 + b); SB(); }
      }
#pragma unroll
      for (int b = 0; b < 5; b++) {
        const f32x2 r = bc2(__builtin_amdgcn_rcpf(L[b][0]));
        *(LAS u32x2*)(ob0 + b * 256) = (u32x2){pk2(lo2(O0[b]) * r), pk2(hi2(O0[b]) * r)};
        *(LAS u32x2*)(ob1 + b * o1s) = (u32x2){pk2(lo2(O1[b]) * r), pk2(hi2(O1[b]) * r)};
      }
      WAIT_LGKM0(); BAR();
#pragma unroll
      for (int i = 0; i < 4; i++) {
        const int n = tid + 512 * i;
        if (i < 3 || (A && w == 0)) {
          const int kg = (n * 6554) >> 20, sl = n - kg * 160;
          const u32x4 v = *(LAS u32x4*)(smem + O_OFF0 + n * 16 + (kg >> 3) * (O_OFF1 - O_OFF0 - 8 * 2560));
          st128(v, wsr, t0 + sl < tend ? (kg * 2048 + t0 + sl) * 16 : OOB, WS_H + grp * 10 * 2048 * 16);
        }
      }
      ST(0, 5);
      __builtin_amdgcn_s_waitcnt(0x0F70);
    }
    wait_vm<0>(); WAIT_LGKM0(); BAR();
    ST(0, 6);
    if (!A && w == 7) __builtin_amdgcn_raw_buffer_store_b64(tagv, wsr, l == 0 ? 0 : OOB, (ffn ? WS_FF : WS_FA) + tile * 128 + grp * 8, 16);
  }
  relane(); geo();
  if constexpr (!A) {
    if (w == 7) {
      const int r0 = tt * 64, r1 = r0 + 63;
      const int fa = (3 * r0 + 2) >> 9, fb = (3 * r1 + 2) >> 9, aa = r0 / 160, ab = r1 / 160;
      const int lf = l < 12 ? fa : fb, la = l < 32 ? aa : ab, jf = l < 12 ? l : l - 12;
      const int of = WS_FF + lf * 128 + jf * 8, oa = WS_FA + la * 128 + (l & 7) * 8;
      const int off = l < 24 ? of : oa;
#pragma unroll 1
      for (unsigned n = 0; n < SPIN_CAP; n++) {
        u32x2 f;
        asm volatile("global_load_dwordx2 %0, %1, %2 sc1\n\ts_waitcnt vmcnt(0)" : "=&v"(f) : "v"(off), "s"(WS) : "memory");
        if (__builtin_amdgcn_ballot_w64(((f[0] ^ tagv[0]) | (f[1] ^ tagv[1])) != 0) == 0) break;
        __builtin_amdgcn_s_sleep(1);
      }
    }
  } else {
#pragma unroll
    for (int i = 0; i < 5; i++) w2_dma(7, i, slotL3);
  }
  ST(0, 7);
  BAR();
  ST(0, 8); ST(3, 1);
  f32x4 ac2[4][5];
#pragma unroll
  for (int b = 0; b < 4; b++)
#pragma unroll
    for (int T = 0; T < 5; T++) ac2[b][T] = (f32x4){0.f, 0.f, 0.f, 0.f};
  u32x4 hf[NS][4];
  const int hvo = (g * 2048 + tt * 64 + c) * 16;
  auto h_load1 = [&](int j, int b) { hf[j][b] = __builtin_amdgcn_raw_buffer_load_b128(wsr, hvo, WS_H + ks_of(j) * 4 * 2048 * 16 + ((b + s + 1) & 3) * 256, 16); };
  u32x2 xres[5];
  const int ooff = row_off(tt * 64 + s * 16 + c) + (ct * 80 + g * 4) * 2;
#define HD 2
#pragma unroll
  for (int j = 0; j < HD; j++)
#pragma unroll
    for (int b = 0; b < 4; b++) h_load1(j, b);
  SB();
  sfor<0, NS>([&](auto jc) {
    constexpr int j = decltype(jc)::value;
    ST(3, 2 + j);
    if (j == 0) __builtin_amdgcn_s_setprio(3);
    if (j == 2) __builtin_amdgcn_s_setprio(2);
    if (j == 4) __builtin_amdgcn_s_setprio(1);
    if (j == (A ? 6 : 7)) __builtin_amdgcn_s_setprio(0);
    constexpr bool reg = j < 4 || j == NS - 1;
    u32x4 wf[5];
    if constexpr (!reg) {
      const int sl = j == 4 ? slotL0 : j == 5 ? slotL1 : j == 6 ? slotL2 : slotL3;
      asm volatile("" ::: "memory");
#pragma unroll
      for (int T = 0; T < 5; T++) wf[T] = *(LAS u32x4*)(smem + sl + T * 1024 + l * 16);
    }
    if constexpr (A && j == NS - 1) { BAR(); }
#pragma unroll
    for (int m = 0; m < 20; m++) {
      const int b = m / 5, T = m % 5;
      if (T == 0 && j + HD < NS) h_load1(j + HD, b);
      ac2[b][T] = mfma(reg ? Wr[j < 4 ? j : 4][T] : wf[T], hf[j][b], ac2[b][T]);
      if (A && j == NS - 2 && (m & 3) == 1) xres[m >> 2] = *(const u32x2*)(X + ooff + (m >> 2) * 32);
      if (j == NS - 1 && m >= 5) {
        const int bb = (m - 5) / 5;
        *(LAS f32x4*)(smem + (bb == 0 ? slotL0 : bb == 1 ? slotL1 : slotL2) + T * 1024 + l * 16) = ac2[bb][T];
      }
      SB();
    }
  });
  ST(0, 9); ST(3, 11);
  auto gather = [&](int i) {
    const int v = (s + i) & 3;
    const int sv = i == 3 ? (A ? 4 + v : (v < 3 ? 16 + v : 26)) : i == 2 ? (A ? 8 + v : (v < 3 ? 19 + v : 27)) : (A ? v : 28 + v);
    LAS char* pp = smem + sv * 5120 + l * 16;
#pragma unroll
    for (int T = 0; T < 5; T++) ac2[3][T] += *(LAS f32x4*)(pp + T * 1024);
  };
  if constexpr (!A) {
    WAIT_LGKM0(); BAR();
#pragma unroll
    for (int i = 1; i < 4; i++) gather(i);
#pragma unroll
    for (int T = 0; T < 5; T++) *(LAS f32x4*)(smem + slotL3 + T * 1024 + l * 16) = ac2[3][T];
    WAIT_LGKM0(); BAR();
  } else {
    WAIT_LGKM0(); BAR();
    ST(0, 10); ST(3, 12);
#pragma unroll
    for (int i = 1; i < 4; i++) gather(i);
#pragma unroll
    for (int T = 0; T < 5; T++) {
      f32x4 a = ac2[3][T] + *(LAS f32x4*)(smem + (22 + s) * 5120 + T * 1024 + l * 16);
      a[0] += __uint_as_float(xres[T][0] << 16); a[1] += __uint_as_float(xres[T][0] & 0xffff0000u);
      a[2] += __uint_as_float(xres[T][1] << 16); a[3] += __uint_as_float(xres[T][1] & 0xffff0000u);
      *(u32x2*)(Out + ooff + T * 32) = (u32x2){pk2(lo2(a)), pk2(hi2(a))};
    }
  }
}

__global__ __launch_bounds__(512, 2) void k_fused(const char* __restrict__ X, const char* __restrict__ Wqkv, const char* __restrict__ Wo,
                                                  const char* __restrict__ Wg, const char* __restrict__ Wu, const char* __restrict__ Wd,
                                                  char* __restrict__ Out, char* __restrict__ WS, const Roles roles) {
  __shared__ __attribute__((aligned(16))) char smem_[LDS_BYTES];
  const char* code;
  asm volatile("s_getpc_b64 %0" : "=s"(code));
#define STARG
  const unsigned role_w = roles.r[blockIdx.x];
  asm volatile("" ::"s"(X), "s"(Wqkv), "s"(Wo), "s"(Wg), "s"(Wu), "s"(Wd), "s"(Out), "s"(WS), "s"(role_w));

  {
    unsigned idx = (blockIdx.x + 64) & 255;
#pragma unroll
    for (int i = 0; i < 4; i++) {
      unsigned v = roles.r[idx];
      asm volatile("" : "+s"(v));
      idx = (idx + 48 + (v >> 31)) & 255;
    }
    asm volatile("" ::"s"(idx));
  }
  const int w = __builtin_amdgcn_readfirstlane(threadIdx.x >> 6);
  if (w < 4) body<true>(X, Wqkv, Wo, Wg, Wu, Wd, Out, WS, role_w, (LAS char*)smem_, w, code STARG);
  else body<false>(X, Wqkv, Wo, Wg, Wu, Wd, Out, WS, role_w, (LAS char*)smem_, w, code STARG);
}

extern "C" void kernel_launch(void* const* d_in, const int* in_sizes, int n_in, void* d_out, int out_size, void* d_ws, size_t ws_size,
                              hipStream_t stream) {
  Roles roles;
  for (int b = 0; b < 256; b++) {
    const int xj = b & 7, s32 = b >> 3, odd = xj & 1;
    int t0 = 0, n = 0, grp = 0, tile = 0;
    if (s32 < 18) {
      grp = 3 * (xj >> 1) + s32 % 3; tile = 6 * odd + s32 / 3;
      t0 = 512 * tile / 3; n = 512 * (tile + 1) / 3 - t0;
    } else if (s32 < 31) {
      const int idx = s32 - 18;
      grp = (xj & 6) + (idx >= 7 - odd); tile = odd ? (idx >= 6 ? idx : idx + 7) : (idx >= 7 ? idx - 7 : idx);
      t0 = 160 * tile; n = t0 + 160 > 2048 ? 2048 - t0 : 160;
    }
    roles.r[b] = t0 | n << 11 | grp << 19 | tile << 23;
  }
  k_fused<<<256, 512, 0, stream>>>((const char*)d_in[0], (const char*)d_in[1], (const char*)d_in[2], (const char*)d_in[4], (const char*)d_in[5],
                                   (const char*)d_in[6], (char*)d_out, (char*)d_ws, roles);
}
```

```cpp
#include <hip/hip_runtime.h>
#include <stdint.h>
#include <type_traits>

#define LAS __attribute__((address_space(3)))
#define DI __device__ __forceinline__
typedef short s16x4 __attribute__((ext_vector_type(4)));
typedef __bf16 b16x2 __attribute__((ext_vector_type(2)));
typedef __bf16 b16x8 __attribute__((ext_vector_type(8)));
typedef float f32x2 __attribute__((ext_vector_type(2)));
typedef float f32x4 __attribute__((ext_vector_type(4)));
typedef unsigned u32x2 __attribute__((ext_vector_type(2)));
typedef unsigned u32x4 __attribute__((ext_vector_type(4)));
typedef unsigned long long u64;

constexpr int LDS_BYTES = 163840;
constexpr int XBUF = 20480, EB0 = 61440, EBUF = 1536, JUNK = 64512, WB0 = 65536, WBUF = 16384, WHALF = 49152;
constexpr int V_OFF0 = 61440, V_OFF1 = 112640, V_HEAD = 176 * 48;
constexpr int CS_OFF = V_OFF0 + 2 * V_HEAD + 2048;
constexpr int O_OFF0 = V_OFF0, O_OFF1 = V_OFF1;
constexpr int WS_E = 4096, WS_FF = 8192, WS_FA = 12288, WS_ST = 16384, WS_W2 = 65536, WS_H = 4194304, WS_BYTES = WS_H + 272 * 2048 * 16;
constexpr int OOB = 0x7FFFFFF0;
constexpr unsigned SPIN_CAP = 1u << 16;
#define CODE_LINES 12

template <int I, int N, class F> DI void sfor(F&& f) {
  if constexpr (I < N) { f(std::integral_constant<int, I>{}); sfor<I + 1, N>(f); }
}
DI unsigned pk2(float a, float b) { f32x2 v = {a, b}; return __builtin_bit_cast(unsigned, __builtin_convertvector(v, b16x2)); }
DI unsigned pk2(f32x2 v) { return __builtin_bit_cast(unsigned, __builtin_convertvector(v, b16x2)); }
DI f32x2 lo2(f32x4 v) { return __builtin_shufflevector(v, v, 0, 1); }
DI f32x2 hi2(f32x4 v) { return __builtin_shufflevector(v, v, 2, 3); }
DI f32x2 bc2(float v) { return (f32x2){v, v}; }
DI f32x2 fma2(f32x2 a, f32x2 b, f32x2 c) { return __builtin_elementwise_fma(a, b, c); }
DI f32x4 mfma(b16x8 a, b16x8 b, f32x4 c) { return __builtin_amdgcn_mfma_f32_16x16x32_bf16(a, b, c, 0, 0, 0); }
DI f32x4 mfma(u32x4 a, u32x4 b, f32x4 c) { return mfma(__builtin_bit_cast(b16x8, a), __builtin_bit_cast(b16x8, b), c); }
DI void mfma_acc(f32x4& c, b16x8 a, b16x8 b) { asm volatile("v_mfma_f32_16x16x32_bf16 %0, %1, %2, %0" : "+v"(c) : "v"(a), "v"(b)); }
DI b16x8 lds_b128(LAS char* p) { return *(LAS b16x8*)p; }
DI s16x4 lds_tr(LAS char* p) { return __builtin_amdgcn_ds_read_tr16_b64_v4i16((LAS s16x4*)p); }
DI b16x8 cat(s16x4 a, s16x4 b) { return __builtin_bit_cast(b16x8, __builtin_shufflevector(a, b, 0, 1, 2, 3, 4, 5, 6, 7)); }
DI b16x8 lds_tr2(LAS char* p, int d) { return cat(lds_tr(p), lds_tr(p + d)); }
DI void dmas(unsigned voff, const char* sb, unsigned dst) {
  asm volatile("s_mov_b32 m0, %2\n\ts_nop 0\n\tglobal_load_lds_dwordx4 %0, %1" ::"v"(voff), "s"(sb), "s"(dst) : "memory");
}
DI void dmas_sc1(unsigned voff, const char* sb, unsigned dst) {
  asm volatile("s_mov_b32 m0, %2\n\ts_nop 0\n\tglobal_load_lds_dwordx4 %0, %1 sc1" ::"v"(voff), "s"(sb), "s"(dst) : "memory");
}
DI void dmas_masked(unsigned voff, const char* sb, unsigned dst, u64 mask) {
  asm volatile("s_mov_b32 m0, %2\n\ts_mov_b64 exec, %3\n\tglobal_load_lds_dwordx4 %0, %1\n\ts_mov_b64 exec, -1" ::"v"(voff), "s"(sb), "s"(dst), "s"(mask) : "memory");
}
DI void touch(const char* a) { asm volatile("s_mov_b32 m0, %1\n\ts_nop 0\n\tglobal_load_lds_dword %0, off" ::"v"(a), "n"(JUNK) : "memory"); }
template <class R> DI void st128(u32x4 v, R rsrc, int voff, int soff) {
  __builtin_amdgcn_raw_buffer_store_b128(v, rsrc, voff, soff, 16);
  asm volatile("s_nop 1" ::"v"(v) : "memory");
}
template <int N> DI void wait_vm() { asm volatile("s_waitcnt vmcnt(%0)" ::"n"(N) : "memory"); }
#define WAIT_LGKM0() do { asm volatile("" ::: "memory"); __builtin_amdgcn_s_waitcnt(0xC07F); asm volatile("" ::: "memory"); } while (0)
#define BAR() do { asm volatile("" ::: "memory"); __builtin_amdgcn_s_barrier(); asm volatile("" ::: "memory"); } while (0)
#define SB() __builtin_amdgcn_sched_barrier(0)
DI float fmx(float a, float b) { return __builtin_elementwise_maximum(a, b); }
DI int row_off(int slot) { return slot * 2560 - (slot >> 10) * (2047 * 1280); }
DI float xrow_sum(float v) {
  unsigned u = __float_as_uint(v);
  auto r = __builtin_amdgcn_permlane16_swap(u, u, false, false);
  v = __uint_as_float(r[0]) + __uint_as_float(r[1]);
  u = __float_as_uint(v);
  auto r2 = __builtin_amdgcn_permlane32_swap(u, u, false, false);
  return __uint_as_float(r2[0]) + __uint_as_float(r2[1]);
}
template <int C> DI float dpp_add(float v) { return v + __uint_as_float(__builtin_amdgcn_update_dpp(0, __float_as_uint(v), C, 0xf, 0xf, true)); }
DI float wave_sum(float v) { return xrow_sum(dpp_add<0x121>(dpp_add<0x122>(dpp_add<0x124>(dpp_add<0x128>(v))))); }

#define ST(set, k)

struct Frag { b16x8 x[6]; s16x4 w[4][2]; };
struct Roles { unsigned r[256]; };

template <bool A>
DI void body(const char* __restrict__ X, const char* __restrict__ Wqkv, const char* __restrict__ Wo, const char* __restrict__ Wg,
             const char* __restrict__ Wu, const char* __restrict__ Wd, char* __restrict__ Out, char* __restrict__ WS, const unsigned role_w,
             LAS char* smem, const int w, const char* code
) {
  constexpr int NB = A ? 6 : 5, B0 = A ? 0 : 5, NS = A ? 9 : 8;
  int tid = threadIdx.x, l = tid & 63;
  const int s = w & 3;
  int g = l >> 4, c = l & 15, q = c >> 2, p = l & 3;
  auto relane = [&]() { tid = threadIdx.x; asm volatile("" : "+v"(tid)); l = tid & 63; g = l >> 4; c = l & 15; q = c >> 2; p = l & 3; };
  const int bid = blockIdx.x, xj = bid & 7, s32 = bid >> 3;
  const bool idle = s32 == 31, ffn = s32 < 18;
  const auto wsr = __builtin_amdgcn_make_buffer_rsrc(WS, 0, WS_BYTES, 0x00020000);
  const int t0 = role_w & 2047, tend = t0 + ((role_w >> 11) & 255), grp = (role_w >> 19) & 15, tile = role_w >> 23;

  auto tx_addr = [&]() { return X + row_off(min((xj & 1) * 944 + s32 * 35 + min(l, 34), 2047)); };
  auto tw_addr = [&](int& stride64) {
    const int m = s32 * 42 + min(l, 41), ma = m - 768, ra = (ma * 7282) >> 16, r9 = ma - 9 * ra, mt = (r9 * 11) >> 5;
    const char* f = ((m & 2) ? Wu : Wg) + ((m & 255) >> 2) * 3072 + (3 * (xj >> 1) + (m >> 8)) * 256 + (m & 1) * 128;
    const char* a = Wqkv + ra * 3840 + mt * 1280 + ((5 * (xj >> 1)) >> 1) * 128 + (r9 - 3 * mt) * 128;
    stride64 = m < 768 ? 64 * 3072 : 64 * 3840;
    return m < 768 ? f : a;
  };
#define STRICT1 0
#define TMODE 2
  auto touch1 = [&](int c0) {
    const char* ta;
    if (w & 1) { int st; ta = tw_addr(st); ta += ((w >> 1) + c0) * st; }
    else ta = tx_addr() + ((w >> 1) + c0) * 128;
    touch(ta);
  };
  if (CODE_LINES && A) { if (w == 0) touch(code + (s32 * CODE_LINES + min(l, CODE_LINES - 1)) * 128); }
  if (TMODE == 1) touch1(0);
  auto row_issue = [&](u32x4& sa, unsigned& sb1) {
    asm volatile("global_load_dwordx4 %0, %2, %4\n\tglobal_load_dword %1, %3, %4 offset:1024" : "=&v"(sa), "=&v"(sb1) : "v"(l * 16), "v"(l * 4), "s"(X + row_off(8 * bid + w)) : "memory");
    SB();
  };
  auto row_stats = [&](u32x4 sa, unsigned sb1) {
    float ss = 0.f, qq = 0.f;
#pragma unroll
    for (int i = 0; i < 5; i++) {
      const unsigned d = i < 4 ? sa[i] : sb1;
      const float lo = __uint_as_float(d << 16), hi = __uint_as_float(d & 0xffff0000u);
      ss += lo + hi; qq = fmaf(lo, lo, fmaf(hi, hi, qq));
    }
    ss = wave_sum(ss); qq = wave_sum(qq);
    const float m = ss * (1.f / 640.f);
    const float R = fmaxf(qq * (1.f / 640.f) - m * m, 0.f) + 1e-5f;
    return (u32x2){__float_as_uint(m), __float_as_uint(R)};
  };
  auto st_store = [&](u32x2 v) { __builtin_amdgcn_raw_buffer_store_b64(v, wsr, l == 0 ? 0 : OOB, WS_ST + (8 * bid + w) * 8, 16); };
  u32x2 tagv;
  auto get_tag = [&]() {
    const char* p0 = WS;
    asm volatile("" : "+s"(p0));
    const u64 tag = *(const __attribute__((address_space(4))) u64*)p0 + 0x9E3779B97F4A7C15ull;
    tagv = (u32x2){(unsigned)tag, (unsigned)(tag >> 32)};
  };
  auto u_dma = [&](int f, unsigned dst) {
    const int rho = l >> 1, qq = rho & 3, gg = ((rho >> 2) & 1) | ((rho >> 4) << 1), t2 = (rho >> 3) & 1;
    const int ks = f / 40, c16 = f - ks * 40;
    const char* base = (ks < 20 ? Wo + ks * 40960 : Wd + (ks - 20) * 40960) + c16 * 32;
    dmas(((ks < 20 ? 8 * gg + 4 * t2 : 16 * t2 + 4 * gg) + qq) * 1280 + (l & 1) * 16, base, dst);
  };
  auto u_read = [&](unsigned src) { return __builtin_bit_cast(u32x4, lds_tr2(smem + src + (q + 4 * (g & 1) + 16 * (g >> 1)) * 32 + p * 8, 256)); };
  auto u_store = [&](int f, u32x4 v) {
    const int ks = f / 40, c16 = f - ks * 40, c5 = c16 / 5;
    st128(v, wsr, l * 16, WS_W2 + ((c5 * 68 + ks) * 5 + (c16 - c5 * 5)) * 1024);
  };
  u32x4 e0, e1;
  auto e_issue = [&]() {
    asm volatile("global_load_dwordx4 %0, %2, %3 sc1\n\tglobal_load_dwordx4 %1, %2, %3 offset:16 sc1" : "=&v"(e0), "=&v"(e1) : "v"(l * 32), "s"(WS + WS_E) : "memory");
  };
  auto e_spin = [&]() {
#pragma unroll 1
    for (unsigned n = 0; n < SPIN_CAP; n++) {
      const unsigned d = ((e0[0] ^ tagv[0]) | (e0[1] ^ tagv[1]) | (e0[2] ^ tagv[0]) | (e0[3] ^ tagv[1])) | ((e1[0] ^ tagv[0]) | (e1[1] ^ tagv[1]) | (e1[2] ^ tagv[0]) | (e1[3] ^ tagv[1]));
      if (__builtin_amdgcn_ballot_w64(d != 0) == 0) break;
      __builtin_amdgcn_s_sleep(2);
      e_issue();
      asm volatile("s_waitcnt vmcnt(0)" : "+v"(e0), "+v"(e1)::"memory");
    }
  };
  int tt, ct, ks0, slotL0, slotL1, slotL2, slotL3;
  auto geo = [&]() {
    int s_ = s, b_ = bid;
    asm volatile("" : "+s"(s_), "+s"(b_));
    tt = (b_ & 7) * 4 + (b_ >> 6); ct = (b_ >> 3) & 7;
    ks0 = 17 * s_ + (A ? 0 : 9);
    slotL0 = (A ? 4 + s_ : (s_ < 3 ? 16 + s_ : 26)) * 5120; slotL1 = (A ? 8 + s_ : (s_ < 3 ? 19 + s_ : 27)) * 5120;
    slotL2 = (A ? s_ : 28 + s_) * 5120; slotL3 = (A ? 12 + s_ : 22 + s_) * 5120;
  };
#define ROT 0
  auto ks_of = [&](int j) {
    if (ROT == 0) return ks0 + j;
    const int k = j + 2 * (tt & 3);
    return ks0 + (k >= NS ? k - NS : k);
  };
  auto w2_dma = [&](int j, int i, int slot) { dmas_sc1(l * 16, WS + WS_W2 + ((ct * 68 + ks_of(j)) * 5 + i) * 1024, slot + i * 1024); };
  u32x4 Wr[5][5];
  auto w2_reg1 = [&](int n) { Wr[n / 5][n % 5] = __builtin_amdgcn_raw_buffer_load_b128(wsr, l * 16 + (n % 5) * 1024, WS_W2 + (ct * 68 + ks_of(n < 20 ? n / 5 : NS - 1)) * 5120, 16); };

  if (idle) {
    const int f1 = 2048 + xj * 84 + w;
    relane();
    if (TMODE == 2) touch1(1);
    u32x4 sa; unsigned sb1;
    row_issue(sa, sb1);
    get_tag(); geo();
    u_dma(bid * 8 + w, (65536 + w * 12288));
#pragma unroll 1
    for (int i = 0; i < (A ? 11 : 10); i++) u_dma(f1 + 8 * i, (65536 + w * 12288) + (i + 1) * 1024);
    asm volatile("s_waitcnt vmcnt(0)" : "+v"(sa), "+v"(sb1)::"memory");
    st_store(row_stats(sa, sb1));
    u_store(bid * 8 + w, u_read((65536 + w * 12288)));
#pragma unroll 1
    for (int i = 0; i < (A ? 11 : 10); i++) u_store(f1 + 8 * i, u_read((65536 + w * 12288) + (i + 1) * 1024));
    wait_vm<0>(); WAIT_LGKM0(); BAR();
    if (!A && w == 7) {
      __builtin_amdgcn_raw_buffer_store_b64(tagv, wsr, l == 0 ? 0 : OOB, WS_E + bid * 8, 16);
      e_issue();
      asm volatile("s_waitcnt vmcnt(0)" : "+v"(e0), "+v"(e1)::"memory");
      e_spin();
      if (xj == 0) __builtin_amdgcn_raw_buffer_store_b64(tagv, wsr, l == 0 ? 0 : OOB, 0, 16);
    }
    BAR();
#pragma unroll 1
    for (int i = 0; i < 5; i++) { w2_dma(4, i, slotL0); w2_dma(5, i, slotL1); w2_dma(6, i, slotL2); }
#pragma unroll
    for (int n = 0; n < 25; n++) w2_reg1(n);
  } else {
    u32x4 sa; unsigned sb1;
    row_issue(sa, sb1);
    unsigned vx[3], vw[2], dstx[3];
    const bool ext0 = !A && w >= 6;
    const int wstride = ffn ? 3072 : 3840;
    const u64 mask0 = (!A && w == 6) ? 0xFFFFFF0000000000ull : ~0ull;
    const unsigned vx0 = (l >> 3) * 2560 + (((l & 7) ^ g) << 4);
#pragma unroll
    for (int i = 0; i < 3; i++) {
      const int w3 = 3 * w + i, w14 = 14 + w, eadj = ffn ? -5 : -176;
      const int pc = A ? w3 : i == 0 ? (ext0 ? w14 : w3) : min(w3, 17 + i);
      int s0 = t0 + 8 * pc + (i == 0 && ext0 ? eadj : 0);
      if (!A) asm("s_max_i32 %0, %0, 0\n\ts_min_i32 %0, %0, 0x7f8" : "+s"(s0)::"scc");
      vx[i] = (vx0 ^ ((pc & 1) << 6)) + row_off(s0);
      dstx[i] = pc * 1024 + (i == 0 && ext0 ? EB0 - 512 - 20 * 1024 : 0);
      if (i == 0 && !A) dmas_masked(vx[0], X, dstx[0], mask0); else dmas(vx[i], X, dstx[i]);
      SB();
    }
#pragma unroll
    for (int i = 0; i < 2; i++) {
      const int lc = c ^ (g << 1) ^ (i << 3), la = lc + (A ? 0 : 16);
      const int m1 = la >= 10, m2 = la >= 21;
      const int ca = (m1 + m2) * 1280 + min(la - 10 * m1 - 11 * m2, 9) * 16, cf = lc * 16;
      vw[i] = g * wstride + (ffn ? cf : ca);
    }
    const char* wb = (ffn ? (A ? Wg : Wu) : Wqkv) + (unsigned)(grp * (ffn ? 256 : 160) + 16 * s * wstride);
    const unsigned dstw = WB0 + (A ? 0 : WHALF) + s * 4096;
    auto dslot = [&](int i, int ch, int che) {
      if (i == 0 && !A) dmas_masked(vx[0], X + (ext0 ? che : ch) * 128, dstx[0] + (ext0 ? (che & 1) * EBUF : (ch % 3) * XBUF), mask0);
      else if (i < 3) dmas(vx[i], X + ch * 128, dstx[i] + (ch % 3) * XBUF);
      else dmas(vw[(i - 3) >> 1], wb + (ch * 64 + (i - 3) * 4) * wstride, dstw + (i - 3) * 1024 + (ch % 3) * WBUF);
    };
    ST(2, 1);
#pragma unroll
    for (int i = 3; i < 7; i++) dslot(i, 0, 0);
    ST(2, 2);
    const unsigned ust = dstw + 3 * 1024 + 2 * WBUF;
    u_dma(bid * 8 + w, ust);
    if (TMODE == 2) touch1(1);
    get_tag(); geo();
    *(LAS unsigned*)(smem + EB0 + (l >> 5) * EBUF + (l & 31) * 4) = 0x3F803F80u;
    int xr[2], er[2], wr[4];
    {
      const int re = max(c, 4);
      xr[0] = c * 128 + ((g ^ (c >> 1)) << 4) + B0 * 2048; xr[1] = xr[0] ^ 64;
      er[0] = EB0 + (re - 4) * 128 + ((g ^ (re >> 1)) << 4); er[1] = er[0] ^ 64;
      const int f = q | ((g & 1) << 2);
#pragma unroll
      for (int T = 0; T < 4; T++) {
        const unsigned map = T == 0 ? 0x17161514u : T == 1 ? 0x2D2B2A18u : T == 2 ? 0x2E2E2C00u : 0x03020104u;
        const int ua = 5 * s + ((map >> (8 * p)) & 255), uf = 32 * (T >> 1) + 8 * s + 4 * (T & 1) + p;
        const int u = ffn ? uf : ua;
        wr[T] = WB0 + (u >> 5) * WHALF + (8 * g + q) * 256 + ((((u >> 1) & 15) ^ (f << 1)) << 4) + (u & 1) * 8;
      }
    }
    const char* tpx = X; const char* tpw = X; int tst = 0;
    if constexpr (!A) { tpx = tx_addr(); tpw = tw_addr(tst); }
    auto dslot7 = [&](int n, int ch, int che, int tch) {
      const int tc = tch + (TMODE == 2);
      if (TMODE && !A && tc < 10 && w == 7) { if (n == 1) touch(tpx + tc * 128); else touch(tpw + tc * tst); }
      else dslot(n, ch, che);
    };
    ST(2, 3);
    asm volatile("s_waitcnt vmcnt(%2)" : "+v"(sa), "+v"(sb1) : "n"(8 + (TMODE == 2) - STRICT1) : "memory");
    ST(2, 4);
    u32x2 mystat = row_stats(sa, sb1);
    asm volatile("" : "+v"(mystat));
    u32x4 myfrag;
    ST(2, 5);
#pragma unroll
    for (int i = 0; i < 3; i++) dslot(i, 1, 1);
    f32x4 acc[NB][4];
#pragma unroll
    for (int b = 0; b < NB; b++)
#pragma unroll
      for (int T = 0; T < 4; T++) acc[b][T] = (f32x4){0.f, 0.f, 0.f, 0.f};
#pragma unroll
    for (int b = 0; b < NB; b++) asm volatile("" : "+v"(acc[b][0]), "+v"(acc[b][1]), "+v"(acc[b][2]), "+v"(acc[b][3]));
    u32x2 stv[NB];
    constexpr int NEARLY = A ? 10 : 15;

    {
      constexpr int NM = 4 * NB, NR = 8 + NB;
      Frag Fa, Fb;
      auto rd = [&](Frag& F, int ch, int kk, int n) {
        if (n < 8) F.w[n >> 1][n & 1] = lds_tr(smem + wr[n >> 1] + (ch % 3) * WBUF + (kk * 32 + (n & 1) * 4) * 256);
        else if (n < 13) F.x[n - 8] = lds_b128(smem + xr[kk] + (ch % 3) * XBUF + (n - 8) * 2048);
        else F.x[5] = lds_b128(smem + er[kk] + (ch & 1) * EBUF);
      };
      auto step = [&](Frag& C, Frag& N, int ch, int kk, bool doload, int gap, auto&& mem) {
#pragma unroll
        for (int m = 0; m < NM; m++) {
          if (m == 0) { if (kk == 0) __builtin_amdgcn_s_setprio(3); else __builtin_amdgcn_s_setprio(1); }
          if (m == NM / 2) { if (kk == 0) __builtin_amdgcn_s_setprio(2); else __builtin_amdgcn_s_setprio(0); }
          mfma_acc(acc[m >> 2][m & 3], cat(C.w[m & 3][0], C.w[m & 3][1]), C.x[m >> 2]);
          if (doload && m < NR) rd(N, ch, kk, m);
          if (m % gap == gap - 2) mem(m / gap);
          SB();
        }
      };
      ST(2, 6);
      wait_vm<4 + (TMODE == 2)>(); ST(2, 7);
      WAIT_LGKM0(); BAR();
      ST(0, 1); ST(1, 1); ST(2, 8);
#pragma unroll
      for (int n = 0; n < NR; n++) rd(Fa, 0, 0, n);
      SB();
      ST(2, 9);
      step(Fa, Fb, 0, 1, true, A ? 3 : 2, [&](int n) {
        if (n < 4) dslot(n + 3, 1, 1);
        else if (n == 4) { if (!ext0) dslot(0, 2, 0); else dslot(1, 2, 0); }
        else if (n < 7) dslot7(n - 4, 2, 0, 4);
      });
      sfor<1, 10>([&](auto ic) {
        constexpr int i = decltype(ic)::value;
        if (i == 1) ST(2, 10);
        if constexpr (i == 5 && !A) { if (w == 7) wait_vm<7>(); else wait_vm<6>(); }
        else wait_vm<i == 1 ? 3 : i == 3 ? 8 : i == 9 ? (A ? 10 : 12) : 6>();
        if (i == 1) ST(2, 11);
        if constexpr (i == 8 && !A) {
          if (w == 7) { asm volatile("s_waitcnt vmcnt(4)" : "+v"(e0), "+v"(e1)::"memory"); e_spin(); }
        }
        WAIT_LGKM0(); BAR();
        ST(1, 1 + i);
        if (i == 5) ST(0, 2);
        if constexpr (i == 1) { myfrag = u_read(ust); SB(); }
        auto mem = [&](int n) {
          if (i == 1) {
            if (n == 0) dslot(0, 3, 2);
            else if (n < 5) { if (n == 4) asm volatile("" : "+v"(myfrag)); dslot(n + 2, 2, 0); }
            else if (n < 7) dslot7(n - 4, 3, 2, 5);
            else if (n < 11) dslot(n - 4, 3, 2);
          } else if (i <= 7) {
            if (n >= 7) return;
            if (n == 1 || n == 2) {
              if (i <= 5) dslot7(n, i + 2, i + 1, i + 4);
              else if (!A && i == 7) { if (w == 7) { if (n == 1) e_issue(); } else dslot(n, i + 2, i + 1); }
              else dslot(n, i + 2, i + 1);
            } else dslot(n, i + 2, i + 1);
            if (i == 2 && n == 0) { st_store(mystat); u_store(bid * 8 + w, myfrag); }
            if (!A && i == 4 && n == 0) { if (w == 7) __builtin_amdgcn_raw_buffer_store_b64(tagv, wsr, l == 0 ? 0 : OOB, WS_E + bid * 8, 16); }
          } else if (i == 8) {
            if (n == 0) { if (ext0) dslot(0, 0, 9); }
            else if (n < 6) w2_dma(4, n - 1, slotL0);
            else if (n < (A ? 11 : 13)) w2_reg1(n - 6);
          } else {
            if (n < 5) w2_dma(5, n, slotL1);
            else if (n - 5 + (A ? 5 : 7) < NEARLY) w2_reg1(n - 5 + (A ? 5 : 7));
          }
        };
        constexpr int gap = (i == 1 || i >= 8) ? (A ? 4 : 3) : (A ? 6 : 5), per = (i == 1 || i >= 8) ? (A ? 6 : 7) : 4;
        step(Fb, Fa, i, 0, true, gap, [&](int n) { if (n < per) mem(n); });
        step(Fa, Fb, i, 1, true, gap, [&](int n) { if (n < per) mem(n + per); });
      });
      WAIT_LGKM0(); BAR();
      ST(1, 11);
#pragma unroll
      for (int b = 0; b < NB; b++) {
        const int sl = min(max(b == 5 ? t0 + (ffn ? 155 : -16) + c : t0 + 16 * (B0 + b) + c, 0), 2047);
        stv[b] = __builtin_amdgcn_raw_buffer_load_b64(wsr, sl * 8, WS_ST, 16);
      }
      SB();
      step(Fb, Fa, 0, 0, false, 4, [&](int n) { if (n < 5) w2_dma(6, n, slotL2); });
      __builtin_amdgcn_s_setprio(0);
      asm volatile("s_nop 7\n\ts_nop 1");
      SB();
    }
    ST(0, 3); ST(1, 12);
    relane();
    if constexpr (A) {
      const int cw1 = CS_OFF + s * 256 + g * 16, cw2 = V_OFF1 + s * 2048 + l * 16;
      LAS char* cw = smem + (c == 4 ? cw1 : cw2);
#pragma unroll
      for (int T = 0; T < 4; T++) *(LAS f32x4*)(cw + T * 64) = acc[5][T];
    }
    WAIT_LGKM0(); BAR();
    f32x4 cs[4];
#pragma unroll
    for (int T = 0; T < 4; T++) cs[T] = *(LAS f32x4*)(smem + CS_OFF + (s * 4 + T) * 64 + g * 16);
    ST(0, 4);
    int role = s32;
    asm volatile("" : "+s"(role));
    if (role < 18) {
      const int hso = WS_H + (20 + grp * 4 + s) * 4 * 2048 * 16;
      const float c0 = -2.f * 0.7978845608028654f * 1.4426950408889634f, c1 = c0 * 0.044715f;
#pragma unroll
      for (int b = 0; b < NB; b++) {
        asm volatile("" : "+v"(stv[b]));
        const float R = __uint_as_float(stv[b][1]), rs = __builtin_amdgcn_rsqf(R);
        const f32x2 nm = bc2(-__uint_as_float(stv[b][0])), B2 = bc2(c0 * rs), A2 = bc2(c1 * rs * rs * rs), R2 = bc2(R);
        u32x4 o;
#pragma unroll
        for (int h = 0; h < 4; h++) {
          const int t = h >> 1;
          const f32x2 gg = fma2(nm, h & 1 ? hi2(cs[t]) : lo2(cs[t]), h & 1 ? hi2(acc[b][t]) : lo2(acc[b][t]));
          const f32x2 uu = fma2(nm, h & 1 ? hi2(cs[2 + t]) : lo2(cs[2 + t]), h & 1 ? hi2(acc[b][2 + t]) : lo2(acc[b][2 + t]));
          const f32x2 ar = gg * fma2(gg * gg, A2, B2);
          const f32x2 e = {__builtin_amdgcn_exp2f(ar[0]), __builtin_amdgcn_exp2f(ar[1])};
          const f32x2 d = fma2(e, R2, R2);
          const f32x2 r = {__builtin_amdgcn_rcpf(d[0]), __builtin_amdgcn_rcpf(d[1])};
          o[h] = pk2(gg * uu * r);
          const int n = NEARLY + (A ? 3 * b + h : 2 * b + (h >> 1));
          if ((A ? h < 3 : (h & 1)) && n < 25) { SB(); w2_reg1(n); SB(); }
        }
        const int slot = b == 5 ? t0 + 155 + c : t0 + 16 * (B0 + b) + c;
        const bool ok = slot < tend && (b != 5 || c >= 5);
        st128(o, wsr, ok ? (g * 2048 + slot) * 16 : OOB, hso);
        SB();
      }
#pragma unroll
      for (int b = 0; b < NB; b++)
#pragma unroll
        for (int T = 0; T < 4; T++) asm volatile("" : "=v"(acc[b][T]));
      ST(0, 5);
      __builtin_amdgcn_s_waitcnt(0x0F70);
    }
    asm volatile("" : "+s"(role));
    if (role >= 18) {
      u32x4 Kf[6], Qf[5];
      float cq[5];
      const int vq1 = (0x3105 >> (4 * g)) & 15, vq2 = (0x5425 >> (4 * g)) & 15;
      const int vreg = s < 2 ? V_OFF0 : V_OFF1, voff = vreg + (s & 1) * V_HEAD, kx = vreg + 2 * V_HEAD + (s & 1) * 1024 + l * 16;
#pragma unroll
      for (int b = 0; b < NB; b++) {
        const int vb = b == 5 ? 0 : B0 + b + 1;
        asm volatile("" : "+v"(stv[b]));
        const float rs = __builtin_amdgcn_rsqf(__uint_as_float(stv[b][1]));
        const f32x2 nm = bc2(-__uint_as_float(stv[b][0])), r2 = bc2(rs), f2 = bc2(g == 0 ? 1.f : rs);
        u32x2 P[4];
#pragma unroll
        for (int T = 0; T < 4; T++) {
          if (b == 5 && T == 3) break;
          f32x2 ya = fma2(nm, lo2(cs[T]), lo2(acc[b][T])), yb = fma2(nm, hi2(cs[T]), hi2(acc[b][T]));
          if (T < 2) { ya *= r2; yb *= r2; }
          if (T == 2) { ya *= f2; yb *= f2; }
          P[T] = (u32x2){pk2(ya), pk2(yb)};
          if (T == 1 && b < 5) { SB(); w2_reg1(NEARLY + b); SB(); }
        }
        LAS char* vrow = smem + voff + (vb * 16 + c) * 48;
        *(LAS u32x2*)(vrow + vq1 * 8) = P[1];
        *(LAS u32x2*)(vrow + vq2 * 8) = P[2];
        Kf[b] = (u32x4){P[0][0], P[0][1], g == 0 ? P[1][0] : 0u, g == 0 ? P[1][1] : 0u};
        if (b < 5) {
          Qf[b] = (u32x4){g == 0 ? P[2][0] : P[3][0], g == 0 ? P[2][1] : P[3][1], P[3][0], P[3][1]};
          cq[b] = rs * (0.22360679774997896f * 1.4426950408889634f);
          asm volatile("" : "+v"(Qf[b]), "+v"(cq[b]));
        }
        asm volatile("" : "+v"(Kf[b]));
        SB();
      }
      if constexpr (A) *(LAS u32x4*)(smem + kx) = Kf[4];
      WAIT_LGKM0(); BAR();
      if constexpr (!A) Kf[5] = *(LAS u32x4*)(smem + kx);
      const bool usePrev = 4 * g + 3 >= c + 9;
      f32x4 Cm;
#pragma unroll
      for (int j = 0; j < 4; j++) {
        const int kk = 4 * g + j - (usePrev ? 16 : 0);
        Cm[j] = (unsigned)(c - kk) < 8u ? 0.f : -1e30f;
      }
      LAS char* vbase = smem + voff + (B0 * 16 + 4 * g + q) * 48;
      const int Q0 = 5 * s + g, Q1 = 5 * s + 4;
      LAS char* ob0 = smem + O_OFF0 + (Q0 >> 1) * 2560 + (Q0 >> 4) * (O_OFF1 - O_OFF0 - 8 * 2560) + (Q0 & 1) * 8 + (B0 * 16 + c) * 16;
      const int o1 = O_OFF0 + (Q1 >> 1) * 2560 + (Q1 >> 4) * (O_OFF1 - O_OFF0 - 8 * 2560) + (Q1 & 1) * 8 + (B0 * 16 + c) * 16, o1j = V_OFF1 + 8192 + tid * 8;
      const int o1s = g == 0 ? 256 : 0;
      LAS char* ob1 = smem + (g == 0 ? o1 : o1j);
      f32x4 Sp[5], Sc[5];
#pragma unroll
      for (int b = 0; b < 5; b++) { Sc[b] = mfma(Kf[b], Qf[b], Cm); if (b > 0) Sp[b] = mfma(Kf[b - 1], Qf[b], Cm); }
      Sp[0] = mfma(Kf[5], Qf[0], Cm);
      s16x4 Vt[6][2];
#pragma unroll
      for (int b = 0; b < 6; b++) { Vt[b][0] = lds_tr(vbase + b * 16 * 48 + p * 8); Vt[b][1] = lds_tr(vbase + b * 16 * 48 + 32); }
      SB();
      f32x4 sv[5];
      float mx[5];
#pragma unroll
      for (int b = 0; b < 5; b++) {
        if (((t0 + 16 * (B0 + b)) & 1023) == 0) Sp[b] = (f32x4){-1e30f, -1e30f, -1e30f, -1e30f};
#pragma unroll
        for (int j = 0; j < 4; j++) sv[b][j] = usePrev ? Sp[b][j] : Sc[b][j];
        mx[b] = fmx(fmx(fmx(sv[b][0], sv[b][1]), sv[b][2]), sv[b][3]);
      }
#pragma unroll
      for (int b = 0; b < 5; b++) { auto r = __builtin_amdgcn_permlane16_swap(__float_as_uint(mx[b]), __float_as_uint(mx[b]), false, false); mx[b] = fmx(__uint_as_float(r[0]), __uint_as_float(r[1])); }
#pragma unroll
      for (int b = 0; b < 5; b++) { auto r = __builtin_amdgcn_permlane32_swap(__float_as_uint(mx[b]), __float_as_uint(mx[b]), false, false); mx[b] = fmx(__uint_as_float(r[0]), __uint_as_float(r[1])); }
      u32x4 Pf[5];
#pragma unroll
      for (int b = 0; b < 5; b++) {
        const f32x2 cq2 = bc2(cq[b]), mc = bc2(-mx[b] * cq[b]);
        const f32x2 aa = fma2(lo2(sv[b]), cq2, mc), ab = fma2(hi2(sv[b]), cq2, mc);
        const unsigned p0 = pk2(__builtin_amdgcn_exp2f(aa[0]), __builtin_amdgcn_exp2f(aa[1])), p1 = pk2(__builtin_amdgcn_exp2f(ab[0]), __builtin_amdgcn_exp2f(ab[1]));
        Pf[b] = (u32x4){usePrev ? p0 : 0u, usePrev ? p1 : 0u, usePrev ? 0u : p0, usePrev ? 0u : p1};
        SB(); w2_reg1(NEARLY + 5 + b); SB();
      }
      WAIT_LGKM0(); BAR();
      const f32x4 z = {0.f, 0.f, 0.f, 0.f};
      const u32x4 ones = {0x3F803F80u, 0x3F803F80u, 0x3F803F80u, 0x3F803F80u};
      f32x4 O0[5], O1[5], L[5];
#pragma unroll
      for (int b = 0; b < 5; b++) {
        L[b] = mfma(ones, Pf[b], z);
        O0[b] = mfma(cat(Vt[b][0], Vt[b + 1][0]), __builtin_bit_cast(b16x8, Pf[b]), z);
        O1[b] = mfma(cat(Vt[b][1], Vt[b + 1][1]), __builtin_bit_cast(b16x8, Pf[b]), z);
        if constexpr (A) { SB(); w2_reg1(20 + b); SB(); }
      }
#pragma unroll
      for (int b = 0; b < 5; b++) {
        const f32x2 r = bc2(__builtin_amdgcn_rcpf(L[b][0]));
        *(LAS u32x2*)(ob0 + b * 256) = (u32x2){pk2(lo2(O0[b]) * r), pk2(hi2(O0[b]) * r)};
        *(LAS u32x2*)(ob1 + b * o1s) = (u32x2){pk2(lo2(O1[b]) * r), pk2(hi2(O1[b]) * r)};
      }
      WAIT_LGKM0(); BAR();
#pragma unroll
      for (int i = 0; i < 4; i++) {
        const int n = tid + 512 * i;
        if (i < 3 || (A && w == 0)) {
          const int kg = (n * 6554) >> 20, sl = n - kg * 160;
          const u32x4 v = *(LAS u32x4*)(smem + O_OFF0 + n * 16 + (kg >> 3) * (O_OFF1 - O_OFF0 - 8 * 2560));
          st128(v, wsr, t0 + sl < tend ? (kg * 2048 + t0 + sl) * 16 : OOB, WS_H + grp * 10 * 2048 * 16);
        }
      }
      ST(0, 5);
      __builtin_amdgcn_s_waitcnt(0x0F70);
    }
    wait_vm<0>(); WAIT_LGKM0(); BAR();
    ST(0, 6);
    if (!A && w == 7) __builtin_amdgcn_raw_buffer_store_b64(tagv, wsr, l == 0 ? 0 : OOB, (ffn ? WS_FF : WS_FA) + tile * 128 + grp * 8, 16);
  }
  relane(); geo();
  if constexpr (!A) {
    if (w == 7) {
      const int r0 = tt * 64, r1 = r0 + 63;
      const int fa = (3 * r0 + 2) >> 9, fb = (3 * r1 + 2) >> 9, aa = r0 / 160, ab = r1 / 160;
      const int lf = l < 12 ? fa : fb, la = l < 32 ? aa : ab, jf = l < 12 ? l : l - 12;
      const int of = WS_FF + lf * 128 + jf * 8, oa = WS_FA + la * 128 + (l & 7) * 8;
      const int off = l < 24 ? of : oa;
#pragma unroll 1
      for (unsigned n = 0; n < SPIN_CAP; n++) {
        u32x2 f;
        asm volatile("global_load_dwordx2 %0, %1, %2 sc1\n\ts_waitcnt vmcnt(0)" : "=&v"(f) : "v"(off), "s"(WS) : "memory");
        if (__builtin_amdgcn_ballot_w64(((f[0] ^ tagv[0]) | (f[1] ^ tagv[1])) != 0) == 0) break;
        __builtin_amdgcn_s_sleep(1);
      }
    }
  } else {
#pragma unroll
    for (int i = 0; i < 5; i++) w2_dma(7, i, slotL3);
  }
  ST(0, 7);
  BAR();
  ST(0, 8); ST(3, 1);
  f32x4 ac2[4][5];
#pragma unroll
  for (int b = 0; b < 4; b++)
#pragma unroll
    for (int T = 0; T < 5; T++) ac2[b][T] = (f32x4){0.f, 0.f, 0.f, 0.f};
  u32x4 hf[NS][4];
  const int hvo = (g * 2048 + tt * 64 + c) * 16;
  auto h_load1 = [&](int j, int b) { hf[j][b] = __builtin_amdgcn_raw_buffer_load_b128(wsr, hvo, WS_H + ks_of(j) * 4 * 2048 * 16 + ((b + s + 1) & 3) * 256, 16); };
  u32x2 xres[5];
  const int ooff = row_off(tt * 64 + s * 16 + c) + (ct * 80 + g * 4) * 2;
#define HD 2
#pragma unroll
  for (int j = 0; j < HD; j++)
#pragma unroll
    for (int b = 0; b < 4; b++) h_load1(j, b);
  SB();
  sfor<0, NS>([&](auto jc) {
    constexpr int j = decltype(jc)::value;
    ST(3, 2 + j);
    if (j == 0) __builtin_amdgcn_s_setprio(3);
    if (j == 2) __builtin_amdgcn_s_setprio(2);
    if (j == 4) __builtin_amdgcn_s_setprio(1);
    if (j == (A ? 6 : 7)) __builtin_amdgcn_s_setprio(0);
    constexpr bool reg = j < 4 || j == NS - 1;
    u32x4 wf[5];
    if constexpr (!reg) {
      const int sl = j == 4 ? slotL0 : j == 5 ? slotL1 : j == 6 ? slotL2 : slotL3;
      asm volatile("" ::: "memory");
#pragma unroll
      for (int T = 0; T < 5; T++) wf[T] = *(LAS u32x4*)(smem + sl + T * 1024 + l * 16);
    }
    if constexpr (A && j == NS - 1) { BAR(); }
#pragma unroll
    for (int m = 0; m < 20; m++) {
      const int b = m / 5, T = m % 5;
      if (T == 0 && j + HD < NS) h_load1(j + HD, b);
      ac2[b][T] = mfma(reg ? Wr[j < 4 ? j : 4][T] : wf[T], hf[j][b], ac2[b][T]);
      if (A && j == NS - 2 && (m & 3) == 1) xres[m >> 2] = *(const u32x2*)(X + ooff + (m >> 2) * 32);
      if (j == NS - 1 && m >= 5) {
        const int bb = (m - 5) / 5;
        *(LAS f32x4*)(smem + (bb == 0 ? slotL0 : bb == 1 ? slotL1 : slotL2) + T * 1024 + l * 16) = ac2[bb][T];
      }
      SB();
    }
  });
  ST(0, 9); ST(3, 11);
  auto gather = [&](int i) {
    const int v = (s + i) & 3;
    const int sv = i == 3 ? (A ? 4 + v : (v < 3 ? 16 + v : 26)) : i == 2 ? (A ? 8 + v : (v < 3 ? 19 + v : 27)) : (A ? v : 28 + v);
    LAS char* pp = smem + sv * 5120 + l * 16;
#pragma unroll
    for (int T = 0; T < 5; T++) ac2[3][T] += *(LAS f32x4*)(pp + T * 1024);
  };
  if constexpr (!A) {
    WAIT_LGKM0(); BAR();
#pragma unroll
    for (int i = 1; i < 4; i++) gather(i);
#pragma unroll
    for (int T = 0; T < 5; T++) *(LAS f32x4*)(smem + slotL3 + T * 1024 + l * 16) = ac2[3][T];
    WAIT_LGKM0(); BAR();
  } else {
    WAIT_LGKM0(); BAR();
    ST(0, 10); ST(3, 12);
#pragma unroll
    for (int i = 1; i < 4; i++) gather(i);
#pragma unroll
    for (int T = 0; T < 5; T++) {
      f32x4 a = ac2[3][T] + *(LAS f32x4*)(smem + (22 + s) * 5120 + T * 1024 + l * 16);
      a[0] += __uint_as_float(xres[T][0] << 16); a[1] += __uint_as_float(xres[T][0] & 0xffff0000u);
      a[2] += __uint_as_float(xres[T][1] << 16); a[3] += __uint_as_float(xres[T][1] & 0xffff0000u);
      *(u32x2*)(Out + ooff + T * 32) = (u32x2){pk2(lo2(a)), pk2(hi2(a))};
    }
  }
}

__global__ __launch_bounds__(512, 2) void k_fused(const char* __restrict__ X, const char* __restrict__ Wqkv, const char* __restrict__ Wo,
                                                  const char* __restrict__ Wg, const char* __restrict__ Wu, const char* __restrict__ Wd,
                                                  char* __restrict__ Out, char* __restrict__ WS, const Roles roles) {
  __shared__ __attribute__((aligned(16))) char smem_[LDS_BYTES];
  const char* code;
  asm volatile("s_getpc_b64 %0" : "=s"(code));
#define STARG
  const unsigned role_w = roles.r[blockIdx.x];
  asm volatile("" ::"s"(X), "s"(Wqkv), "s"(Wo), "s"(Wg), "s"(Wu), "s"(Wd), "s"(Out), "s"(WS), "s"(role_w));
  const int w = __builtin_amdgcn_readfirstlane(threadIdx.x >> 6);
  if (w < 4) body<true>(X, Wqkv, Wo, Wg, Wu, Wd, Out, WS, role_w, (LAS char*)smem_, w, code STARG);
  else body<false>(X, Wqkv, Wo, Wg, Wu, Wd, Out, WS, role_w, (LAS char*)smem_, w, code STARG);
}

extern "C" void kernel_launch(void* const* d_in, const int* in_sizes, int n_in, void* d_out, int out_size, void* d_ws, size_t ws_size,
                              hipStream_t stream) {
  Roles roles;
  for (int b = 0; b < 256; b++) {
    const int xj = b & 7, s32 = b >> 3, odd = xj & 1;
    int t0 = 0, n = 0, grp = 0, tile = 0;
    if (s32 < 18) {
      grp = 3 * (xj >> 1) + s32 % 3; tile = 6 * odd + s32 / 3;
      t0 = 512 * tile / 3; n = 512 * (tile + 1) / 3 - t0;
    } else if (s32 < 31) {
      const int idx = s32 - 18;
      grp = (xj & 6) + (idx >= 7 - odd); tile = odd ? (idx >= 6 ? idx : idx + 7) : (idx >= 7 ? idx - 7 : idx);
      t0 = 160 * tile; n = t0 + 160 > 2048 ? 2048 - t0 : 160;
    }
    roles.r[b] = t0 | n << 11 | grp << 19 | tile << 23;
  }
  k_fused<<<256, 512, 0, stream>>>((const char*)d_in[0], (const char*)d_in[1], (const char*)d_in[2], (const char*)d_in[4], (const char*)d_in[5],
                                   (const char*)d_in[6], (char*)d_out, (char*)d_ws, roles);
}
```
